```python
import torch

def _softmax(x, axis=-1):
    return torch.softmax(x, dim=axis)

def _gelu(x):
    return torch.nn.functional.gelu(x, approximate='tanh')

def _silu(x):
    return torch.nn.functional.silu(x)

def _sigmoid(x):
    return torch.sigmoid(x)

def _layernorm(x, axis=-1, eps=1e-05):
    m = x.mean(dim=axis, keepdim=True)
    v = x.var(dim=axis, keepdim=True, unbiased=False)
    return (x - m) / torch.sqrt(v + eps)

def _scatter_add(tgt, idx, src, axis=0):
    return tgt.clone().index_add_(axis, idx, src)

def _segment_sum(src, seg, n):
    out = torch.zeros((n,) + src.shape[1:], dtype=src.dtype)
    return out.index_add_(0, seg, src)

def _pad(x, pw):
    flat = tuple((v for pair in reversed(pw) for v in pair))
    return torch.nn.functional.pad(x, flat)
kernel_name = 'standalone__tree_speculative_verify_tree_attention_over_candidate_token_tree__decode'

def setup_inputs():

    _w = lambda a: (lambda t: t.to(torch.bfloat16) if t.is_floating_point() else t)(torch.as_tensor(a))
    B, Hq, Hkv, T, C, D = (2, 32, 4, 1, 16384, 128)
    tree_mask = torch.zeros((B, T, T), dtype=float)
    for b in range(B):
        parent = torch.zeros(T, dtype=torch.int64)
        for i in range(1, T):
            parent[i] = int(torch.randint(0, i, (1,)).item())
        for i in range(T):
            j = i
            tree_mask[b, i, j] = 1.0
            while j > 0:
                j = parent[j]
                tree_mask[b, i, j] = 1.0
    return {'q': _w((lambda *s: torch.randn(*s, dtype=torch.bfloat16) * 0.8)(B, Hq, T, D)), 'k_cache': _w((lambda *s: torch.randn(*s, dtype=torch.bfloat16) * 0.8)(B, Hkv, C, D)), 'v_cache': _w((lambda *s: torch.randn(*s, dtype=torch.bfloat16) * 0.05)(B, Hkv, C, D)), 'k_tree': _w((lambda *s: torch.randn(*s, dtype=torch.bfloat16) * 0.8)(B, Hkv, T, D)), 'v_tree': _w((lambda *s: torch.randn(*s, dtype=torch.bfloat16) * 0.05)(B, Hkv, T, D)), 'tree_mask': _w(tree_mask)}

def reference(q, k_cache, v_cache, k_tree, v_tree, tree_mask):
    B, Hq, T, D = q.shape
    Hkv, C = (k_cache.shape[1], k_cache.shape[2])
    rep = Hq // Hkv
    kc = (lambda x, n, axis: torch.repeat_interleave(x, n, dim=axis))(k_cache, rep, axis=1)
    vc = (lambda x, n, axis: torch.repeat_interleave(x, n, dim=axis))(v_cache, rep, axis=1)
    kt = (lambda x, n, axis: torch.repeat_interleave(x, n, dim=axis))(k_tree, rep, axis=1)
    vt = (lambda x, n, axis: torch.repeat_interleave(x, n, dim=axis))(v_tree, rep, axis=1)
    k = torch.concatenate([kc, kt], axis=2)
    v = torch.concatenate([vc, vt], axis=2)
    scale = D ** (-0.5)
    scores = torch.einsum('bhqd,bhkd->bhqk', q, k) * scale
    ctx_mask = torch.ones((B, 1, T, C), dtype=scores.dtype)
    tm = tree_mask[:, None, :, :].clone()
    full_mask = torch.concatenate([ctx_mask, tm], axis=-1)
    scores = scores + (1.0 - full_mask) * -1000000000.0
    p = _softmax(scores, axis=-1)
    return torch.einsum('bhqk,bhkd->bhqd', p, v)
```

```cpp
#include <hip/hip_runtime.h>
#include <stdint.h>

typedef __attribute__((ext_vector_type(4))) unsigned u32x4;
typedef __attribute__((ext_vector_type(8))) __bf16 bf16x8;
typedef __attribute__((ext_vector_type(4))) float f32x4;
typedef __attribute__((ext_vector_type(2))) float f32x2;

#define KV_NT 0

constexpr int NB = 2, HQ = 32, HKV = 4, CTX = 16384, D = 128, REP = 8;
constexpr int NGROUP = NB * HKV;
constexpr int NSPLIT = 32;
constexpr int KEYS_WG = CTX / NSPLIT;
constexpr int NWAVE = 8;
constexpr int KEYS_WAVE = KEYS_WG / NWAVE;
constexpr float C2 = 0.088388347648318440f * 1.4426950408889634f;

__device__ __forceinline__ u32x4 ldg(const void* p) {
  return *reinterpret_cast<const u32x4*>(p);
}
__device__ __forceinline__ f32x4 mfma16(u32x4 a, u32x4 b, f32x4 c) {
  return __builtin_amdgcn_mfma_f32_16x16x32_bf16(__builtin_bit_cast(bf16x8, a), __builtin_bit_cast(bf16x8, b), c, 0, 0, 0);
}
__device__ __forceinline__ unsigned cvtpk(float lo, float hi) {
  typedef __attribute__((ext_vector_type(2))) __bf16 bf16x2;
  bf16x2 r = {(__bf16)lo, (__bf16)hi};
  return __builtin_bit_cast(unsigned, r);
}
__device__ __forceinline__ float xrow16_max(float x) {
  auto s = __builtin_amdgcn_permlane16_swap(__float_as_uint(x), __float_as_uint(x), false, false);
  x = fmaxf(__uint_as_float(s[0]), __uint_as_float(s[1]));
  auto t = __builtin_amdgcn_permlane32_swap(__float_as_uint(x), __float_as_uint(x), false, false);
  return fmaxf(__uint_as_float(t[0]), __uint_as_float(t[1]));
}
__device__ __forceinline__ float xrow16_sum(float x) {
  auto s = __builtin_amdgcn_permlane16_swap(__float_as_uint(x), __float_as_uint(x), false, false);
  x = __uint_as_float(s[0]) + __uint_as_float(s[1]);
  auto t = __builtin_amdgcn_permlane32_swap(__float_as_uint(x), __float_as_uint(x), false, false);
  return __uint_as_float(t[0]) + __uint_as_float(t[1]);
}
__device__ __forceinline__ float bf2f(unsigned short u) { return __uint_as_float((unsigned)u << 16); }

constexpr int OSTR = 132;

__global__ void __launch_bounds__(512, 2) split_kernel(const unsigned short* __restrict__ q, const unsigned short* __restrict__ kc,
                                                       const unsigned short* __restrict__ vc, float* __restrict__ pml, float* __restrict__ po) {
  __shared__ __attribute__((aligned(16))) float lds[NWAVE * REP * OSTR + NWAVE * REP * 2];
  const int bid = blockIdx.x, group = bid & 7, split = bid >> 3;
  const int tid = threadIdx.x, lane = tid & 63, wave = __builtin_amdgcn_readfirstlane(tid >> 6), g = lane >> 4, c = lane & 15;
  const size_t key0 = (size_t)group * CTX + split * KEYS_WG + wave * KEYS_WAVE;
  u32x4 Q[4];
  {
    const unsigned short* qp = q + (size_t)(group * REP + (c & 7)) * D + g * 8;
#pragma unroll
    for (int s = 0; s < 4; ++s) Q[s] = *reinterpret_cast<const u32x4*>(qp + s * 32);
  }
  u32x4 K[4][4], V[2][8];
  {
    const unsigned short* kp = kc + (key0 + 4 * (c & 3) + (c >> 2)) * D + g * 8;
#pragma unroll
    for (int t = 0; t < 4; ++t)
#pragma unroll
      for (int s = 0; s < 4; ++s) K[t][s] = ldg(kp + t * 16 * D + s * 32);
    const unsigned short* vp = vc + (key0 + g) * D + c * 8;
#pragma unroll
    for (int u = 0; u < 2; ++u)
#pragma unroll
      for (int j = 0; j < 8; ++j) V[u][j] = ldg(vp + (32 * u + 4 * j) * D);
  }
  __builtin_amdgcn_sched_barrier(0);
  f32x4 S[4];
#pragma unroll
  for (int t = 0; t < 4; ++t) {
    f32x4 a = {0.f, 0.f, 0.f, 0.f};
#pragma unroll
    for (int s = 0; s < 4; ++s) a = mfma16(K[t][s], Q[s], a);
    S[t] = a;
  }
  float m = S[0][0];
#pragma unroll
  for (int t = 0; t < 4; ++t)
#pragma unroll
    for (int r = 0; r < 4; ++r) m = fmaxf(m, S[t][r]);
  m = xrow16_max(m);
  const float mc = m * C2;
  float l = 0.f;
  u32x4 P[2];
#pragma unroll
  for (int u = 0; u < 2; ++u) {
    float p[8];
#pragma unroll
    for (int j = 0; j < 8; ++j) { p[j] = __builtin_amdgcn_exp2f(fmaf(S[2 * u + (j >> 2)][j & 3], C2, -mc)); l += p[j]; }
#pragma unroll
    for (int i = 0; i < 4; ++i) P[u][i] = cvtpk(p[2 * i], p[2 * i + 1]);
  }
  l = xrow16_sum(l);
  f32x4 acc[8];
#pragma unroll
  for (int e = 0; e < 8; ++e) acc[e] = f32x4{0.f, 0.f, 0.f, 0.f};
#pragma unroll
  for (int u = 0; u < 2; ++u)
#pragma unroll
    for (int e = 0; e < 8; ++e) {
      u32x4 A;
#pragma unroll
      for (int i = 0; i < 4; ++i) A[i] = __builtin_amdgcn_perm(V[u][2 * i + 1][e >> 1], V[u][2 * i][e >> 1], (e & 1) ? 0x07060302u : 0x05040100u);
      acc[e] = mfma16(A, P[u], acc[e]);
    }
  float* ml = lds + NWAVE * REP * OSTR;
  if (c < 8) {
    float* o = lds + (wave * REP + c) * OSTR + 32 * g;
#pragma unroll
    for (int r = 0; r < 4; ++r) {
      *reinterpret_cast<f32x4*>(o + 8 * r) = f32x4{acc[0][r], acc[1][r], acc[2][r], acc[3][r]};
      *reinterpret_cast<f32x4*>(o + 8 * r + 4) = f32x4{acc[4][r], acc[5][r], acc[6][r], acc[7][r]};
    }
    if (g == 0) *reinterpret_cast<f32x2*>(ml + (wave * REP + c) * 2) = f32x2{mc, l};
  }
  __syncthreads();
  {
    const int h = wave;
    float mw[NWAVE], lw[NWAVE];
#pragma unroll
    for (int w = 0; w < NWAVE; ++w) { f32x2 t = *reinterpret_cast<const f32x2*>(ml + (w * REP + h) * 2); mw[w] = t[0]; lw[w] = t[1]; }
    float M = mw[0];
#pragma unroll
    for (int w = 1; w < NWAVE; ++w) M = fmaxf(M, mw[w]);
    float L = 0.f, o0 = 0.f, o1 = 0.f;
#pragma unroll
    for (int w = 0; w < NWAVE; ++w) {
      const float wt = __builtin_amdgcn_exp2f(mw[w] - M);
      const f32x2 ov = *reinterpret_cast<const f32x2*>(lds + (w * REP + h) * OSTR + 2 * lane);
      L = fmaf(wt, lw[w], L); o0 = fmaf(wt, ov[0], o0); o1 = fmaf(wt, ov[1], o1);
    }
    const int row = group * REP + h;
    *reinterpret_cast<f32x2*>(po + ((size_t)row * NSPLIT + split) * D + 2 * lane) = f32x2{o0, o1};
    if (lane == 0) *reinterpret_cast<f32x2*>(pml + ((size_t)row * NSPLIT + split) * 2) = f32x2{M, L};
  }
}

__global__ void __launch_bounds__(64) combine_kernel(const unsigned short* __restrict__ q, const unsigned short* __restrict__ kt,
                                                     const unsigned short* __restrict__ vt, const unsigned short* __restrict__ tm,
                                                     const float* __restrict__ pml, const float* __restrict__ po, unsigned short* __restrict__ out) {
  const int row = blockIdx.x, lane = threadIdx.x;
  const int group = row >> 3, b = row >> 5;
  const unsigned qq = *reinterpret_cast<const unsigned*>(q + (size_t)row * D + 2 * lane);
  const unsigned kk = *reinterpret_cast<const unsigned*>(kt + (size_t)group * D + 2 * lane);
  const unsigned vv = *reinterpret_cast<const unsigned*>(vt + (size_t)group * D + 2 * lane);
  const float msk = bf2f(tm[b]);
  f32x2 mlv = {-INFINITY, 0.f};
  if (lane < NSPLIT) mlv = *reinterpret_cast<const f32x2*>(pml + ((size_t)row * NSPLIT + lane) * 2);
  f32x2 ov[NSPLIT];
#pragma unroll
  for (int s = 0; s < NSPLIT; ++s) ov[s] = *reinterpret_cast<const f32x2*>(po + ((size_t)row * NSPLIT + s) * D + 2 * lane);
  float st = __uint_as_float(qq << 16) * __uint_as_float(kk << 16) + __uint_as_float(qq & 0xffff0000u) * __uint_as_float(kk & 0xffff0000u);
#pragma unroll
  for (int o = 32; o >= 1; o >>= 1) st += __shfl_xor(st, o);
  const float mt = st * C2 + (1.0f - msk) * (-1.0e9f * 1.4426950408889634f);
  float M = mlv[0];
#pragma unroll
  for (int o = 32; o >= 1; o >>= 1) M = fmaxf(M, __shfl_xor(M, o));
  M = fmaxf(M, mt);
  const float wl = (lane < NSPLIT) ? __builtin_amdgcn_exp2f(mlv[0] - M) : 0.f;
  float L = wl * mlv[1];
#pragma unroll
  for (int o = 32; o >= 1; o >>= 1) L += __shfl_xor(L, o);
  const float wt = __builtin_amdgcn_exp2f(mt - M);
  L += wt;
  float o0 = wt * __uint_as_float(vv << 16), o1 = wt * __uint_as_float(vv & 0xffff0000u);
#pragma unroll
  for (int s = 0; s < NSPLIT; ++s) {
    const float w = __shfl(wl, s);
    o0 = fmaf(w, ov[s][0], o0); o1 = fmaf(w, ov[s][1], o1);
  }
  const float inv = 1.0f / L;
  *reinterpret_cast<unsigned*>(out + (size_t)row * D + 2 * lane) = cvtpk(o0 * inv, o1 * inv);
}

extern "C" void kernel_launch(void* const* d_in, const int* in_sizes, int n_in, void* d_out, int out_size, void* d_ws, size_t ws_size,
                              hipStream_t stream) {
  const unsigned short* q = (const unsigned short*)d_in[0];
  const unsigned short* kc = (const unsigned short*)d_in[1];
  const unsigned short* vc = (const unsigned short*)d_in[2];
  const unsigned short* kt = (const unsigned short*)d_in[3];
  const unsigned short* vt = (const unsigned short*)d_in[4];
  const unsigned short* tm = (const unsigned short*)d_in[5];
  float* pml = (float*)d_ws;
  float* po = (float*)((char*)d_ws + 65536);
  split_kernel<<<NGROUP * NSPLIT, 512, 0, stream>>>(q, kc, vc, pml, po);
  combine_kernel<<<NB * HQ, 64, 0, stream>>>(q, kt, vt, tm, pml, po, (unsigned short*)d_out);
}
```

```cpp
#include <hip/hip_runtime.h>
#include <stdint.h>
#define STAMP_DECL
#define STAMP(k)

typedef __attribute__((ext_vector_type(4))) unsigned u32x4;
typedef __attribute__((ext_vector_type(2))) unsigned u32x2;
typedef __attribute__((ext_vector_type(8))) __bf16 bf16x8;
typedef __attribute__((ext_vector_type(4))) float f32x4;
typedef __attribute__((ext_vector_type(2))) float f32x2;
#define LAS __attribute__((address_space(3)))
#define GAS __attribute__((address_space(1)))

#define K_NT 1
#define V_NT 0
#define KFIRST 0
#define POLL_FIRST 16
#define POLL_SLEEP 4
#define K_POL " nt"

constexpr int D = 128, CTX = 16384, REP = 8;
constexpr int NSPLIT = 32, KEYS_WG = 512, NWAVE = 8, KEYS_WAVE = 64;
constexpr float C2 = 0.088388347648318440f * 1.4426950408889634f;
constexpr unsigned KV_BYTES = 2u * 4u * CTX * D * 2u;
constexpr int WREG = 16384;
constexpr int OSTR = 132;
constexpr int LDS_STAT = NWAVE * WREG;
constexpr unsigned WS_GROUP = 256u * 1024u, WS_REC = 4096u;
constexpr unsigned SPIN_CAP = 1u << 18;
constexpr unsigned TAGM = 0x3ffffu;

__device__ __forceinline__ f32x4 mfma16(u32x4 a, u32x4 b, f32x4 c) {
  return __builtin_amdgcn_mfma_f32_16x16x32_bf16(__builtin_bit_cast(bf16x8, a), __builtin_bit_cast(bf16x8, b), c, 0, 0, 0);
}
__device__ __forceinline__ unsigned cvtpk(float lo, float hi) {
  typedef __attribute__((ext_vector_type(2))) __bf16 bf16x2;
  bf16x2 r = {(__bf16)lo, (__bf16)hi};
  return __builtin_bit_cast(unsigned, r);
}
__device__ __forceinline__ float xrow16_max(float x) {
  auto s = __builtin_amdgcn_permlane16_swap(__float_as_uint(x), __float_as_uint(x), false, false);
  x = fmaxf(__uint_as_float(s[0]), __uint_as_float(s[1]));
  auto t = __builtin_amdgcn_permlane32_swap(__float_as_uint(x), __float_as_uint(x), false, false);
  return fmaxf(__uint_as_float(t[0]), __uint_as_float(t[1]));
}
__device__ __forceinline__ float xrow16_sum(float x) {
  auto s = __builtin_amdgcn_permlane16_swap(__float_as_uint(x), __float_as_uint(x), false, false);
  x = __uint_as_float(s[0]) + __uint_as_float(s[1]);
  auto t = __builtin_amdgcn_permlane32_swap(__float_as_uint(x), __float_as_uint(x), false, false);
  return __uint_as_float(t[0]) + __uint_as_float(t[1]);
}
template <int CTRL> __device__ __forceinline__ float dppf(float x) {
  return __builtin_bit_cast(float, __builtin_amdgcn_mov_dpp(__builtin_bit_cast(int, x), CTRL, 0xf, 0xf, true));
}
template <int CTRL> __device__ __forceinline__ unsigned dppu(unsigned x) { return (unsigned)__builtin_amdgcn_mov_dpp((int)x, CTRL, 0xf, 0xf, true); }
constexpr int XOR1 = 0xB1, XOR2 = 0x4E, HMIR = 0x141, ROR8 = 0x128;
__device__ __forceinline__ float row16_sum(float x) { x += dppf<XOR1>(x); x += dppf<XOR2>(x); x += dppf<HMIR>(x); x += dppf<ROR8>(x); return x; }
__device__ __forceinline__ float bflo(unsigned u) { return __uint_as_float(u << 16); }
__device__ __forceinline__ float bfhi(unsigned u) { return __uint_as_float(u & 0xffff0000u); }

__global__ void __launch_bounds__(576, 3) tree_attn_decode(const unsigned short* __restrict__ q, const unsigned short* __restrict__ kc,
                                                           const unsigned short* __restrict__ vc, const unsigned short* __restrict__ kt,
                                                           const unsigned short* __restrict__ vt, const unsigned short* __restrict__ tm,
                                                           unsigned short* __restrict__ out, unsigned char* __restrict__ ws) {
  __shared__ __attribute__((aligned(16))) unsigned char smem[NWAVE * WREG + 640];
  STAMP_DECL
  STAMP(0);
  const int bid = blockIdx.x, group = bid & 7, split = bid >> 3;
  const int tid = threadIdx.x, lane = tid & 63, wave = __builtin_amdgcn_readfirstlane(tid >> 6), g = lane >> 4, c = lane & 15;
  const __amdgpu_buffer_rsrc_t rw = __builtin_amdgcn_make_buffer_rsrc((void*)(ws + (size_t)group * WS_GROUP), 0, (int)WS_GROUP, 0x00020000);
  float* stat = reinterpret_cast<float*>(smem + LDS_STAT);

  if (wave == NWAVE) {
    const int ch = split >> 2, cq = split & 3, p = lane & 7, rec = lane >> 3;
    const unsigned long long gen = __hip_atomic_load((GAS unsigned long long*)(ws + (size_t)group * WS_GROUP), __ATOMIC_RELAXED, __HIP_MEMORY_SCOPE_AGENT);
    const unsigned tq = *reinterpret_cast<const unsigned*>(q + (size_t)(group * REP + ch) * D + 2 * lane);
    const unsigned tk = *reinterpret_cast<const unsigned*>(kt + (size_t)group * D + 2 * lane);
    const int dout = 32 * (p & 3) + 8 * (rec & 3) + 2 * cq + (p >> 2);
    const unsigned tv = vt[(size_t)group * D + dout];
    const unsigned tmask = tm[group >> 2];
    unsigned short* outp = out + (size_t)(group * REP + ch) * D + dout;
    float st = bflo(tq) * bflo(tk) + bfhi(tq) * bfhi(tk);
    st = xrow16_sum(row16_sum(st));
    float mt = st * C2 + (1.0f - bflo(tmask)) * (-1.0e9f * 1.4426950408889634f);
    float tvf = bflo(tv);
    const unsigned tag = __builtin_amdgcn_readfirstlane(((unsigned)gen + 1u) & TAGM);
    asm volatile("" : "+v"(tvf), "+v"(mt));
    asm volatile("s_barrier" ::: "memory");
    STAMP(4);
    __builtin_amdgcn_s_sleep(POLL_FIRST);
    u32x4 o[4];
    bool good = false;
    for (unsigned spin = 0; spin < SPIN_CAP; ++spin) {
      asm volatile("" ::: "memory");
#pragma unroll
      for (int k = 0; k < 4; ++k) o[k] = __builtin_amdgcn_raw_buffer_load_b128(rw, WS_REC + split * (NSPLIT * 128) + lane * 16 + k * 1024, 0, 16);
      unsigned bad = 0;
#pragma unroll
      for (int k = 0; k < 4; ++k) bad |= (o[k][0] ^ tag) | (o[k][2] ^ tag);
      if (__all((bad & TAGM) == 0)) { good = true; break; }
      __builtin_amdgcn_s_sleep(POLL_SLEEP);
    }
    float lse[4], M = mt;
#pragma unroll
    for (int k = 0; k < 4; ++k) {
      unsigned x = (((o[k][0] >> 18) & 3u) | ((o[k][2] >> 16) & 12u)) << (4 * p);
      x |= dppu<XOR1>(x); x |= dppu<XOR2>(x); x |= dppu<HMIR>(x);
      lse[k] = __uint_as_float(x);
      M = fmaxf(M, lse[k]);
    }
    M = fmaxf(M, dppf<ROR8>(M));
    M = xrow16_max(M);
    float L = 0.f, a0 = 0.f, a1 = 0.f, a2 = 0.f, a3 = 0.f;
#pragma unroll
    for (int k = 0; k < 4; ++k) {
      const float w = __builtin_amdgcn_exp2f(lse[k] - M);
      L += w;
      a0 = fmaf(w, __uint_as_float(o[k][1] & 0xfffffc00u), a0);
      a1 = fmaf(w, __uint_as_float(__builtin_amdgcn_alignbit(o[k][1], o[k][0], 10) & 0xfffffc00u), a1);
      a2 = fmaf(w, __uint_as_float(o[k][3] & 0xfffffc00u), a2);
      a3 = fmaf(w, __uint_as_float(__builtin_amdgcn_alignbit(o[k][3], o[k][2], 10) & 0xfffffc00u), a3);
    }
    L += dppf<ROR8>(L); a0 += dppf<ROR8>(a0); a1 += dppf<ROR8>(a1); a2 += dppf<ROR8>(a2); a3 += dppf<ROR8>(a3);
    L = xrow16_sum(L); a0 = xrow16_sum(a0); a1 = xrow16_sum(a1); a2 = xrow16_sum(a2); a3 = xrow16_sum(a3);
    const float wt = __builtin_amdgcn_exp2f(mt - M);
    const float sel = (rec & 2) ? ((rec & 1) ? a3 : a2) : ((rec & 1) ? a1 : a0);
    const float inv = good ? 1.0f / (L + wt) : __builtin_nanf("");
    const float res = fmaf(wt, tvf, sel) * inv;
    if (rec < 4) *outp = (unsigned short)cvtpk(res, res);
    if (split == 0 && lane == 0 && good)
      __hip_atomic_store((GAS unsigned long long*)(ws + (size_t)group * WS_GROUP), gen + 1ull, __ATOMIC_RELAXED, __HIP_MEMORY_SCOPE_AGENT);
    STAMP(6);
    return;
  }

  const unsigned kvbase = (unsigned)(group * CTX + split * KEYS_WG + wave * KEYS_WAVE) * (D * 2);
  const __amdgpu_buffer_rsrc_t rk = __builtin_amdgcn_make_buffer_rsrc((void*)kc, 0, (int)KV_BYTES, 0x00020000);
  const __amdgpu_buffer_rsrc_t rv = __builtin_amdgcn_make_buffer_rsrc((void*)vc, 0, (int)KV_BYTES, 0x00020000);
  const unsigned lds0 = (unsigned)(uintptr_t)(LAS unsigned char*)smem + (unsigned)wave * WREG;
  {
    const unsigned v0 = g * 256 + ((c ^ (4 * g + 0)) << 4), v1 = g * 256 + ((c ^ (4 * g + 1)) << 4), v2 = g * 256 + ((c ^ (4 * g + 2)) << 4), v3 = g * 256 + ((c ^ (4 * g + 3)) << 4);
#pragma unroll
    for (int t = 0; t < 4; ++t)
      asm volatile("s_mov_b32 m0, %0\n\ts_nop 0\n\t"
                   "buffer_load_dwordx4 %1, %5, %6 offen" K_POL " lds\n\t"
                   "buffer_load_dwordx4 %2, %5, %6 offen offset:1024" K_POL " lds\n\t"
                   "buffer_load_dwordx4 %3, %5, %6 offen offset:2048" K_POL " lds\n\t"
                   "buffer_load_dwordx4 %4, %5, %6 offen offset:3072" K_POL " lds"
                   :: "s"(lds0 + t * 4096), "v"(v0), "v"(v1), "v"(v2), "v"(v3), "s"(rk), "s"(kvbase + t * 4096) : "m0", "memory");
  }
  u32x4 Q[4];
  {
    const unsigned short* qp = q + (size_t)(group * REP + (c & 7)) * D + g * 8;
#pragma unroll
    for (int s = 0; s < 4; ++s) Q[s] = *reinterpret_cast<const u32x4*>(qp + s * 32);
  }
  const unsigned long long gen = __hip_atomic_load((GAS unsigned long long*)(ws + (size_t)group * WS_GROUP), __ATOMIC_RELAXED, __HIP_MEMORY_SCOPE_AGENT);
  __builtin_amdgcn_sched_barrier(0);
  u32x4 V[16];
#pragma unroll
  for (int n = 0; n < 16; ++n) V[n] = __builtin_amdgcn_raw_buffer_load_b128(rv, lane * 16 + (n & 3) * 1024, kvbase + (n >> 2) * 4096, V_NT ? 2 : 0);
  __builtin_amdgcn_sched_barrier(0);
  asm volatile("s_waitcnt vmcnt(16)" ::: "memory");
  __builtin_amdgcn_sched_barrier(0);
  f32x4 S[4];
  {
    const int row = 4 * (c & 3) + (c >> 2);
    const unsigned char* kb = smem + wave * WREG + row * 256;
#pragma unroll
    for (int t = 0; t < 4; ++t) {
      f32x4 a = {0.f, 0.f, 0.f, 0.f};
#pragma unroll
      for (int s = 0; s < 4; ++s) {
        const u32x4 kf = *reinterpret_cast<const u32x4*>(kb + t * 4096 + (((4 * s + g) ^ c) << 4));
        a = mfma16(kf, Q[s], a);
      }
      S[t] = a;
    }
  }
  float m = S[0][0];
#pragma unroll
  for (int t = 0; t < 4; ++t)
#pragma unroll
    for (int r = 0; r < 4; ++r) m = fmaxf(m, S[t][r]);
  m = xrow16_max(m);
  const float mc = m * C2;
  float l = 0.f;
  u32x4 P[2];
#pragma unroll
  for (int u = 0; u < 2; ++u) {
    float p[8];
#pragma unroll
    for (int j = 0; j < 8; ++j) { p[j] = __builtin_amdgcn_exp2f(fmaf(S[2 * u + (j >> 2)][j & 3], C2, -mc)); l += p[j]; }
#pragma unroll
    for (int i = 0; i < 4; ++i) P[u][i] = cvtpk(p[2 * i], p[2 * i + 1]);
  }
  l = xrow16_sum(l);
  if (lane < 8) { stat[lane * NWAVE + wave] = mc; stat[64 + lane * NWAVE + wave] = l; }
  const unsigned tag = __builtin_amdgcn_readfirstlane(((unsigned)gen + 1u) & TAGM);
  __builtin_amdgcn_sched_barrier(0);
  f32x4 acc[8];
#pragma unroll
  for (int u = 0; u < 2; ++u) {
#pragma unroll
    for (int e = 0; e < 8; ++e) {
      u32x4 A;
#pragma unroll
      for (int i = 0; i < 4; ++i) A[i] = __builtin_amdgcn_perm(V[8 * u + 2 * i + 1][e >> 1], V[8 * u + 2 * i][e >> 1], (e & 1) ? 0x07060302u : 0x05040100u);
      acc[e] = mfma16(A, P[u], u == 0 ? f32x4{0.f, 0.f, 0.f, 0.f} : acc[e]);
    }
    __builtin_amdgcn_sched_barrier(0);
  }
  if (c < 8) {
    float* o = reinterpret_cast<float*>(smem + wave * WREG) + c * OSTR + 4 * g;
#pragma unroll
    for (int e = 0; e < 8; ++e) *reinterpret_cast<f32x4*>(o + 16 * e) = acc[e];
  }
  asm volatile("s_waitcnt lgkmcnt(0)\n\ts_barrier" ::: "memory");
  {
    const int h = wave;
    f32x2 ov[NWAVE];
#pragma unroll
    for (int w = 0; w < NWAVE; ++w) ov[w] = *reinterpret_cast<const f32x2*>(smem + w * WREG + (h * OSTR + 2 * lane) * 4);
    const f32x4 m0 = *reinterpret_cast<const f32x4*>(stat + h * NWAVE), m1 = *reinterpret_cast<const f32x4*>(stat + h * NWAVE + 4);
    const f32x4 l0 = *reinterpret_cast<const f32x4*>(stat + 64 + h * NWAVE), l1 = *reinterpret_cast<const f32x4*>(stat + 64 + h * NWAVE + 4);
    const float Mh = fmaxf(fmaxf(fmaxf(m0[0], m0[1]), fmaxf(m0[2], m0[3])), fmaxf(fmaxf(m1[0], m1[1]), fmaxf(m1[2], m1[3])));
    float f[NWAVE];
#pragma unroll
    for (int w = 0; w < 4; ++w) { f[w] = __builtin_amdgcn_exp2f(m0[w] - Mh); f[4 + w] = __builtin_amdgcn_exp2f(m1[w] - Mh); }
    const float L = (fmaf(f[1], l0[1], f[0] * l0[0]) + fmaf(f[3], l0[3], f[2] * l0[2])) + (fmaf(f[5], l1[1], f[4] * l1[0]) + fmaf(f[7], l1[3], f[6] * l1[2]));
    const float inv = __builtin_amdgcn_rcpf(L);
    const unsigned lse = __float_as_uint(Mh + __builtin_amdgcn_logf(L));
    const float o0 = ((fmaf(f[1], ov[1][0], f[0] * ov[0][0]) + fmaf(f[3], ov[3][0], f[2] * ov[2][0])) + (fmaf(f[5], ov[5][0], f[4] * ov[4][0]) + fmaf(f[7], ov[7][0], f[6] * ov[6][0]))) * inv;
    const float o1 = ((fmaf(f[1], ov[1][1], f[0] * ov[0][1]) + fmaf(f[3], ov[3][1], f[2] * ov[2][1])) + (fmaf(f[5], ov[5][1], f[4] * ov[4][1]) + fmaf(f[7], ov[7][1], f[6] * ov[6][1]))) * inv;
    const unsigned a = (__float_as_uint(o0) + 0x200u) & 0xfffffc00u, b = (__float_as_uint(o1) + 0x200u) & 0xfffffc00u;
    const unsigned hi = a | (b >> 22), lo = (b << 10) | (((lse >> (2 * c)) & 3u) << 18) | tag;
    __builtin_amdgcn_raw_buffer_store_b64(u32x2{lo, hi}, rw, WS_REC + ((4 * h + g) * NSPLIT + split) * 128 + c * 8, 0, 16);
  }
}

extern "C" void kernel_launch(void* const* d_in, const int* in_sizes, int n_in, void* d_out, int out_size, void* d_ws, size_t ws_size,
                              hipStream_t stream) {
  tree_attn_decode<<<256, 576, 0, stream>>>((const unsigned short*)d_in[0], (const unsigned short*)d_in[1], (const unsigned short*)d_in[2],
                                            (const unsigned short*)d_in[3], (const unsigned short*)d_in[4], (const unsigned short*)d_in[5],
                                            (unsigned short*)d_out, (unsigned char*)d_ws);
}
```

```cpp
#include <hip/hip_runtime.h>
#include <stdint.h>
#define STAMP_DECL
#define STAMP(k)

typedef __attribute__((ext_vector_type(4))) unsigned u32x4;
typedef __attribute__((ext_vector_type(2))) unsigned u32x2;
typedef __attribute__((ext_vector_type(8))) __bf16 bf16x8;
typedef __attribute__((ext_vector_type(4))) float f32x4;
typedef __attribute__((ext_vector_type(2))) float f32x2;
#define LAS __attribute__((address_space(3)))
#define GAS __attribute__((address_space(1)))

#define K_NT 1
#define V_NT 1
#define ORD 1
#define CMAX 0
#define POLL_FIRST 8
#define POLL_SLEEP 4
#define WS_OFF 0
#define K_POL " nt"

constexpr int D = 128, CTX = 16384, REP = 8;
constexpr int NSPLIT = 32, KEYS_WG = 512, NWAVE = 8;
constexpr float C2 = 0.088388347648318440f * 1.4426950408889634f;
constexpr unsigned KV_BYTES = 2u * 4u * CTX * D * 2u;
constexpr int WREG = 16384;
constexpr int OSTR = 132;
constexpr int LDS_STAT = NWAVE * WREG;
constexpr unsigned PSTR = ORD ? 8192u : 1024u, WSTR = ORD ? 1024u : 16384u;
constexpr unsigned WS_GROUP = 256u * 1024u, WS_REC = 4096u;
constexpr unsigned SPIN_CAP = 1u << 18;
constexpr unsigned TAGM = 0x3ffffu;

__device__ __forceinline__ f32x4 mfma16(u32x4 a, u32x4 b, f32x4 c) {
  return __builtin_amdgcn_mfma_f32_16x16x32_bf16(__builtin_bit_cast(bf16x8, a), __builtin_bit_cast(bf16x8, b), c, 0, 0, 0);
}
__device__ __forceinline__ unsigned cvtpk(float lo, float hi) {
  typedef __attribute__((ext_vector_type(2))) __bf16 bf16x2;
  bf16x2 r = {(__bf16)lo, (__bf16)hi};
  return __builtin_bit_cast(unsigned, r);
}
__device__ __forceinline__ float xrow16_max(float x) {
  auto s = __builtin_amdgcn_permlane16_swap(__float_as_uint(x), __float_as_uint(x), false, false);
  x = fmaxf(__uint_as_float(s[0]), __uint_as_float(s[1]));
  auto t = __builtin_amdgcn_permlane32_swap(__float_as_uint(x), __float_as_uint(x), false, false);
  return fmaxf(__uint_as_float(t[0]), __uint_as_float(t[1]));
}
__device__ __forceinline__ float xrow16_sum(float x) {
  auto s = __builtin_amdgcn_permlane16_swap(__float_as_uint(x), __float_as_uint(x), false, false);
  x = __uint_as_float(s[0]) + __uint_as_float(s[1]);
  auto t = __builtin_amdgcn_permlane32_swap(__float_as_uint(x), __float_as_uint(x), false, false);
  return __uint_as_float(t[0]) + __uint_as_float(t[1]);
}
template <int CTRL> __device__ __forceinline__ float dppf(float x) {
  return __builtin_bit_cast(float, __builtin_amdgcn_mov_dpp(__builtin_bit_cast(int, x), CTRL, 0xf, 0xf, true));
}
template <int CTRL> __device__ __forceinline__ unsigned dppu(unsigned x) { return (unsigned)__builtin_amdgcn_mov_dpp((int)x, CTRL, 0xf, 0xf, true); }
constexpr int XOR1 = 0xB1, XOR2 = 0x4E, HMIR = 0x141, ROR8 = 0x128;
__device__ __forceinline__ float row16_sum(float x) { x += dppf<XOR1>(x); x += dppf<XOR2>(x); x += dppf<HMIR>(x); x += dppf<ROR8>(x); return x; }
__device__ __forceinline__ float bflo(unsigned u) { return __uint_as_float(u << 16); }
__device__ __forceinline__ float bfhi(unsigned u) { return __uint_as_float(u & 0xffff0000u); }

__global__ void __launch_bounds__(576, 3) tree_attn_decode(const unsigned short* __restrict__ q, const unsigned short* __restrict__ kc,
                                                           const unsigned short* __restrict__ vc, const unsigned short* __restrict__ kt,
                                                           const unsigned short* __restrict__ vt, const unsigned short* __restrict__ tm,
                                                           unsigned short* __restrict__ out, unsigned char* __restrict__ ws) {
  __shared__ __attribute__((aligned(16))) unsigned char smem[NWAVE * WREG + 640];
  STAMP_DECL
  STAMP(0);
  const int bid = blockIdx.x, group = bid & 7, split = bid >> 3;
  const int tid = threadIdx.x, lane = tid & 63, wave = __builtin_amdgcn_readfirstlane(tid >> 6), g = lane >> 4, c = lane & 15;
  float* stat = reinterpret_cast<float*>(smem + LDS_STAT);

  constexpr unsigned X = PSTR - 1024u;
  const unsigned v0 = g * 256 + ((c ^ (4 * g + 0)) << 4), v1 = X + g * 256 + ((c ^ (4 * g + 1)) << 4), v2 = 2 * X + g * 256 + ((c ^ (4 * g + 2)) << 4), v3 = 3 * X + g * 256 + ((c ^ (4 * g + 3)) << 4);
  const unsigned kvbase = (unsigned)(group * CTX + split * KEYS_WG) * (D * 2) + (unsigned)wave * WSTR;
  const unsigned lds0 = (unsigned)(uintptr_t)(LAS unsigned char*)smem + (unsigned)wave * WREG;
  asm volatile("" :: "v"(v0), "v"(v1), "v"(v2), "v"(v3), "s"(kvbase), "s"(lds0));
  asm volatile("" :: "s"(q), "s"(kc), "s"(vc), "s"(ws));
  if (__builtin_expect(wave < NWAVE, 1)) {
    const __amdgpu_buffer_rsrc_t rk = __builtin_amdgcn_make_buffer_rsrc((void*)kc, 0, (int)KV_BYTES, 0x00020000);
    {
#pragma unroll
      for (int t = 0; t < 4; ++t)
        asm volatile("s_mov_b32 m0, %0\n\ts_nop 0\n\t"
                     "buffer_load_dwordx4 %1, %5, %6 offen" K_POL " lds\n\t"
                     "buffer_load_dwordx4 %2, %5, %6 offen offset:1024" K_POL " lds\n\t"
                     "buffer_load_dwordx4 %3, %5, %6 offen offset:2048" K_POL " lds\n\t"
                     "buffer_load_dwordx4 %4, %5, %6 offen offset:3072" K_POL " lds"
                     :: "s"(lds0 + t * 4096), "v"(v0), "v"(v1), "v"(v2), "v"(v3), "s"(rk), "s"(kvbase + t * 4 * PSTR) : "m0", "memory");
    }
    u32x4 Q[4];
    {
      const unsigned short* qp = q + (size_t)(group * REP + (c & 7)) * D + g * 8;
#pragma unroll
      for (int s = 0; s < 4; ++s) Q[s] = *reinterpret_cast<const u32x4*>(qp + s * 32);
    }
    __builtin_amdgcn_sched_barrier(0);
    const __amdgpu_buffer_rsrc_t rv = __builtin_amdgcn_make_buffer_rsrc((void*)vc, 0, (int)KV_BYTES, 0x00020000);
    u32x4 V[16];
#pragma unroll
    for (int n = 0; n < 16; ++n) V[n] = __builtin_amdgcn_raw_buffer_load_b128(rv, lane * 16, kvbase + n * PSTR, V_NT ? 2 : 0);
    __builtin_amdgcn_sched_barrier(0);
    asm volatile("s_waitcnt vmcnt(16)" ::: "memory");
    __builtin_amdgcn_sched_barrier(0);
    f32x4 S[4];
    {
      const int row = 4 * (c & 3) + (c >> 2);
      const unsigned char* kb = smem + wave * WREG + row * 256;
#pragma unroll
      for (int t = 0; t < 4; ++t) {
        f32x4 a = {0.f, 0.f, 0.f, 0.f};
#pragma unroll
        for (int s = 0; s < 4; ++s) {
          const u32x4 kf = *reinterpret_cast<const u32x4*>(kb + t * 4096 + (((4 * s + g) ^ c) << 4));
          a = mfma16(kf, Q[s], a);
        }
        S[t] = a;
      }
    }
    float m = S[0][0];
#pragma unroll
    for (int t = 0; t < 4; ++t)
#pragma unroll
      for (int r = 0; r < 4; ++r) m = fmaxf(m, S[t][r]);
    m = xrow16_max(m);
    const float mc = m * C2;
    float l = 0.f;
    u32x4 P[2];
#pragma unroll
    for (int u = 0; u < 2; ++u) {
      float p[8];
#pragma unroll
      for (int j = 0; j < 8; ++j) { p[j] = __builtin_amdgcn_exp2f(fmaf(S[2 * u + (j >> 2)][j & 3], C2, -mc)); l += p[j]; }
#pragma unroll
      for (int i = 0; i < 4; ++i) P[u][i] = cvtpk(p[2 * i], p[2 * i + 1]);
    }
    l = xrow16_sum(l);
    if (lane < 8) { stat[lane * NWAVE + wave] = mc; stat[64 + lane * NWAVE + wave] = l; }
    __builtin_amdgcn_sched_barrier(0);
    f32x4 acc[8];
#pragma unroll
    for (int u = 0; u < 2; ++u) {
#pragma unroll
      for (int e = 0; e < 8; ++e) {
        u32x4 A;
#pragma unroll
        for (int i = 0; i < 4; ++i) A[i] = __builtin_amdgcn_perm(V[8 * u + 2 * i + 1][e >> 1], V[8 * u + 2 * i][e >> 1], (e & 1) ? 0x07060302u : 0x05040100u);
        acc[e] = mfma16(A, P[u], u == 0 ? f32x4{0.f, 0.f, 0.f, 0.f} : acc[e]);
      }
#pragma unroll
      for (int e = 0; e < 8; ++e) { __builtin_amdgcn_sched_group_barrier(0x2, 4, 0); __builtin_amdgcn_sched_group_barrier(0x8, 1, 0); }
      __builtin_amdgcn_sched_barrier(0);
    }
    if (c < 8) {
      float* o = reinterpret_cast<float*>(smem + wave * WREG) + c * OSTR + 4 * g;
#pragma unroll
      for (int e = 0; e < 8; ++e) *reinterpret_cast<f32x4*>(o + 16 * e) = acc[e];
    }
    asm volatile("s_waitcnt lgkmcnt(0)\n\ts_barrier" ::: "memory");
    {
      const int h = wave;
      const __amdgpu_buffer_rsrc_t rw = __builtin_amdgcn_make_buffer_rsrc((void*)(ws + (size_t)group * WS_GROUP), 0, (int)WS_GROUP, 0x00020000);
      f32x2 ov[NWAVE];
#pragma unroll
      for (int w = 0; w < NWAVE; ++w) ov[w] = *reinterpret_cast<const f32x2*>(smem + w * WREG + (h * OSTR + 2 * lane) * 4);
      const f32x4 l0 = *reinterpret_cast<const f32x4*>(stat + 64 + h * NWAVE), l1 = *reinterpret_cast<const f32x4*>(stat + 64 + h * NWAVE + 4);
      const unsigned tag = reinterpret_cast<const unsigned*>(stat)[128];
      const f32x4 m0 = *reinterpret_cast<const f32x4*>(stat + h * NWAVE), m1 = *reinterpret_cast<const f32x4*>(stat + h * NWAVE + 4);
      const float Mh = fmaxf(fmaxf(fmaxf(m0[0], m0[1]), fmaxf(m0[2], m0[3])), fmaxf(fmaxf(m1[0], m1[1]), fmaxf(m1[2], m1[3])));
      float f[NWAVE];
#pragma unroll
      for (int w = 0; w < 4; ++w) { f[w] = __builtin_amdgcn_exp2f(m0[w] - Mh); f[4 + w] = __builtin_amdgcn_exp2f(m1[w] - Mh); }
      const float L = (fmaf(f[1], l0[1], f[0] * l0[0]) + fmaf(f[3], l0[3], f[2] * l0[2])) + (fmaf(f[5], l1[1], f[4] * l1[0]) + fmaf(f[7], l1[3], f[6] * l1[2]));
      const float inv = __builtin_amdgcn_rcpf(L);
      const float o0 = ((fmaf(f[1], ov[1][0], f[0] * ov[0][0]) + fmaf(f[3], ov[3][0], f[2] * ov[2][0])) + (fmaf(f[5], ov[5][0], f[4] * ov[4][0]) + fmaf(f[7], ov[7][0], f[6] * ov[6][0]))) * inv;
      const float o1 = ((fmaf(f[1], ov[1][1], f[0] * ov[0][1]) + fmaf(f[3], ov[3][1], f[2] * ov[2][1])) + (fmaf(f[5], ov[5][1], f[4] * ov[4][1]) + fmaf(f[7], ov[7][1], f[6] * ov[6][1]))) * inv;
      const unsigned lse = __float_as_uint(Mh + __builtin_amdgcn_logf(L));
      const unsigned a = (__float_as_uint(o0) + 0x200u) & 0xfffffc00u, b = (__float_as_uint(o1) + 0x200u) & 0xfffffc00u;
      const unsigned hi = a | (b >> 22), lo = (b << 10) | (((lse >> (2 * c)) & 3u) << 18) | tag;
      __builtin_amdgcn_raw_buffer_store_b64(u32x2{lo, hi}, rw, WS_REC + ((4 * h + g) * NSPLIT + split) * 128 + c * 8, 0, 16);
    }
    return;
  }

  {
    const __amdgpu_buffer_rsrc_t rw = __builtin_amdgcn_make_buffer_rsrc((void*)(ws + (size_t)group * WS_GROUP), 0, (int)WS_GROUP, 0x00020000);
    const int ch = split >> 2, cq = split & 3, p = lane & 7, rec = lane >> 3;
    const unsigned long long gen = __hip_atomic_load((GAS unsigned long long*)(ws + (size_t)group * WS_GROUP), __ATOMIC_RELAXED, __HIP_MEMORY_SCOPE_AGENT);
    const unsigned tq = *reinterpret_cast<const unsigned*>(q + (size_t)(group * REP + ch) * D + 2 * lane);
    const unsigned tk = *reinterpret_cast<const unsigned*>(kt + (size_t)group * D + 2 * lane);
    const int dout = 32 * (p & 3) + 8 * (rec & 3) + 2 * cq + (p >> 2);
    const unsigned tv = vt[(size_t)group * D + dout];
    const unsigned tmask = tm[group >> 2];
    GAS unsigned short* outp = (GAS unsigned short*)out + (size_t)(group * REP + ch) * D + dout;
    {
      u32x4 x[4];
#pragma unroll
      for (int k = 0; k < 4; ++k) x[k] = __builtin_amdgcn_raw_buffer_load_b128(rw, WS_REC + split * (NSPLIT * 128) + lane * 16 + k * 1024, 0, 16);
      const unsigned y = __hip_atomic_load(outp, __ATOMIC_RELAXED, __HIP_MEMORY_SCOPE_WORKGROUP);
      asm volatile("" :: "v"(x[0]), "v"(x[1]), "v"(x[2]), "v"(x[3]), "v"(y));
    }
    float st = bflo(tq) * bflo(tk) + bfhi(tq) * bfhi(tk);
    st = xrow16_sum(row16_sum(st));
    float mt = st * C2 + (1.0f - bflo(tmask)) * (-1.0e9f * 1.4426950408889634f);
    float tvf = bflo(tv);
    const unsigned tag = __builtin_amdgcn_readfirstlane(((unsigned)gen + 1u) & TAGM);
    asm volatile("" : "+v"(tvf), "+v"(mt));
    if (lane == 0) reinterpret_cast<unsigned*>(stat)[128] = tag;
    asm volatile("s_waitcnt lgkmcnt(0)\n\ts_barrier" ::: "memory");
    STAMP(4);
    __builtin_amdgcn_s_sleep(POLL_FIRST);
    u32x4 o[4];
    bool good = false;
    for (unsigned spin = 0; spin < SPIN_CAP; ++spin) {
      asm volatile("" ::: "memory");
#pragma unroll
      for (int k = 0; k < 4; ++k) o[k] = __builtin_amdgcn_raw_buffer_load_b128(rw, WS_REC + split * (NSPLIT * 128) + lane * 16 + k * 1024, 0, 16);
      unsigned bad = 0;
#pragma unroll
      for (int k = 0; k < 4; ++k) bad |= (o[k][0] ^ tag) | (o[k][2] ^ tag);
      if (__all((bad & TAGM) == 0)) { good = true; break; }
      __builtin_amdgcn_s_sleep(POLL_SLEEP);
    }
    float lse[4], M = mt;
#pragma unroll
    for (int k = 0; k < 4; ++k) {
      unsigned x = (((o[k][0] >> 18) & 3u) | ((o[k][2] >> 16) & 12u)) << (4 * p);
      x |= dppu<XOR1>(x); x |= dppu<XOR2>(x); x |= dppu<HMIR>(x);
      lse[k] = __uint_as_float(x);
      M = fmaxf(M, lse[k]);
    }
    M = fmaxf(M, dppf<ROR8>(M));
    M = xrow16_max(M);
    float L = 0.f, a0 = 0.f, a1 = 0.f, a2 = 0.f, a3 = 0.f;
#pragma unroll
    for (int k = 0; k < 4; ++k) {
      const float w = __builtin_amdgcn_exp2f(lse[k] - M);
      L += w;
      a0 = fmaf(w, __uint_as_float(o[k][1] & 0xfffffc00u), a0);
      a1 = fmaf(w, __uint_as_float(__builtin_amdgcn_alignbit(o[k][1], o[k][0], 10) & 0xfffffc00u), a1);
      a2 = fmaf(w, __uint_as_float(o[k][3] & 0xfffffc00u), a2);
      a3 = fmaf(w, __uint_as_float(__builtin_amdgcn_alignbit(o[k][3], o[k][2], 10) & 0xfffffc00u), a3);
    }
    L += dppf<ROR8>(L); a0 += dppf<ROR8>(a0); a1 += dppf<ROR8>(a1); a2 += dppf<ROR8>(a2); a3 += dppf<ROR8>(a3);
    L = xrow16_sum(L); a0 = xrow16_sum(a0); a1 = xrow16_sum(a1); a2 = xrow16_sum(a2); a3 = xrow16_sum(a3);
    const float wt = __builtin_amdgcn_exp2f(mt - M);
    const float sel = (rec & 2) ? ((rec & 1) ? a3 : a2) : ((rec & 1) ? a1 : a0);
    const float inv = good ? __builtin_amdgcn_rcpf(L + wt) : __builtin_nanf("");
    const float res = fmaf(wt, tvf, sel) * inv;
    if (rec < 4) *outp = (unsigned short)cvtpk(res, res);
    if (split == 0 && lane == 0 && good)
      __hip_atomic_store((GAS unsigned long long*)(ws + (size_t)group * WS_GROUP), gen + 1ull, __ATOMIC_RELAXED, __HIP_MEMORY_SCOPE_AGENT);
    STAMP(6);
  }
}

extern "C" void kernel_launch(void* const* d_in, const int* in_sizes, int n_in, void* d_out, int out_size, void* d_ws, size_t ws_size,
                              hipStream_t stream) {
  tree_attn_decode<<<256, 576, 0, stream>>>((const unsigned short*)d_in[0], (const unsigned short*)d_in[1], (const unsigned short*)d_in[2],
                                            (const unsigned short*)d_in[3], (const unsigned short*)d_in[4], (const unsigned short*)d_in[5],
                                            (unsigned short*)d_out, (unsigned char*)d_ws + (size_t)(WS_OFF) * (1u << 20));
}
```

```cpp
#include <hip/hip_runtime.h>
#include <stdint.h>
#define STAMP_DECL
#define STAMP(k)

typedef __attribute__((ext_vector_type(4))) unsigned u32x4;
typedef __attribute__((ext_vector_type(2))) unsigned u32x2;
typedef __attribute__((ext_vector_type(8))) __bf16 bf16x8;
typedef __attribute__((ext_vector_type(4))) float f32x4;
typedef __attribute__((ext_vector_type(2))) float f32x2;
#define LAS __attribute__((address_space(3)))
#define GAS __attribute__((address_space(1)))

#define K_NT 1
#define V_NT 1
#define ORD 1
#define CMAX 0
#define KFIRST 0
#define OUT_POL 1
#define POLL_FIRST 24
#define POLL_SLEEP 4
#define WS_OFF 0
#define K_POL " nt"

constexpr int D = 128, CTX = 16384, REP = 8;
constexpr int NSPLIT = 32, KEYS_WG = 512, NWAVE = 8;
constexpr float C2 = 0.088388347648318440f * 1.4426950408889634f;
constexpr unsigned KV_BYTES = 2u * 4u * CTX * D * 2u;
constexpr int WREG = 16384;
constexpr int OSTR = 132;
constexpr int LDS_STAT = NWAVE * WREG;
constexpr unsigned PSTR = ORD ? 8192u : 1024u, WSTR = ORD ? 1024u : 16384u;
constexpr unsigned WS_GROUP = 256u * 1024u, WS_REC = 4096u;
constexpr unsigned SPIN_CAP = 1u << 18;
constexpr unsigned TAGM = 0x3ffffu;

__device__ __forceinline__ f32x4 mfma16(u32x4 a, u32x4 b, f32x4 c) {
  return __builtin_amdgcn_mfma_f32_16x16x32_bf16(__builtin_bit_cast(bf16x8, a), __builtin_bit_cast(bf16x8, b), c, 0, 0, 0);
}
__device__ __forceinline__ unsigned cvtpk(float lo, float hi) {
  typedef __attribute__((ext_vector_type(2))) __bf16 bf16x2;
  bf16x2 r = {(__bf16)lo, (__bf16)hi};
  return __builtin_bit_cast(unsigned, r);
}
__device__ __forceinline__ float xrow16_max(float x) {
  auto s = __builtin_amdgcn_permlane16_swap(__float_as_uint(x), __float_as_uint(x), false, false);
  x = fmaxf(__uint_as_float(s[0]), __uint_as_float(s[1]));
  auto t = __builtin_amdgcn_permlane32_swap(__float_as_uint(x), __float_as_uint(x), false, false);
  return fmaxf(__uint_as_float(t[0]), __uint_as_float(t[1]));
}
__device__ __forceinline__ float xrow16_sum(float x) {
  auto s = __builtin_amdgcn_permlane16_swap(__float_as_uint(x), __float_as_uint(x), false, false);
  x = __uint_as_float(s[0]) + __uint_as_float(s[1]);
  auto t = __builtin_amdgcn_permlane32_swap(__float_as_uint(x), __float_as_uint(x), false, false);
  return __uint_as_float(t[0]) + __uint_as_float(t[1]);
}
template <int CTRL> __device__ __forceinline__ float dppf(float x) {
  return __builtin_bit_cast(float, __builtin_amdgcn_mov_dpp(__builtin_bit_cast(int, x), CTRL, 0xf, 0xf, true));
}
template <int CTRL> __device__ __forceinline__ unsigned dppu(unsigned x) { return (unsigned)__builtin_amdgcn_mov_dpp((int)x, CTRL, 0xf, 0xf, true); }
constexpr int XOR1 = 0xB1, XOR2 = 0x4E, HMIR = 0x141, ROR8 = 0x128;
__device__ __forceinline__ float row16_sum(float x) { x += dppf<XOR1>(x); x += dppf<XOR2>(x); x += dppf<HMIR>(x); x += dppf<ROR8>(x); return x; }
__device__ __forceinline__ float bflo(unsigned u) { return __uint_as_float(u << 16); }
__device__ __forceinline__ float bfhi(unsigned u) { return __uint_as_float(u & 0xffff0000u); }

__global__ void __launch_bounds__(576, 3) tree_attn_decode(const unsigned short* __restrict__ q, const unsigned short* __restrict__ kc,
                                                           const unsigned short* __restrict__ vc, const unsigned short* __restrict__ kt,
                                                           const unsigned short* __restrict__ vt, const unsigned short* __restrict__ tm,
                                                           unsigned short* __restrict__ out, unsigned char* __restrict__ ws) {
  __shared__ __attribute__((aligned(16))) unsigned char smem[NWAVE * WREG + 640];
  STAMP_DECL
  STAMP(0);
  const int bid = blockIdx.x, group = bid & 7, split = bid >> 3;
  const int tid = threadIdx.x, lane = tid & 63, wave = __builtin_amdgcn_readfirstlane(tid >> 6), g = lane >> 4, c = lane & 15;
  float* stat = reinterpret_cast<float*>(smem + LDS_STAT);

  constexpr unsigned X = PSTR - 1024u;
  const unsigned v0 = g * 256 + ((c ^ (4 * g + 0)) << 4), v1 = X + g * 256 + ((c ^ (4 * g + 1)) << 4), v2 = 2 * X + g * 256 + ((c ^ (4 * g + 2)) << 4), v3 = 3 * X + g * 256 + ((c ^ (4 * g + 3)) << 4);
  const unsigned kvbase = (unsigned)(group * CTX + split * KEYS_WG) * (D * 2) + (unsigned)wave * WSTR;
  const unsigned lds0 = (unsigned)(uintptr_t)(LAS unsigned char*)smem + (unsigned)wave * WREG;
  asm volatile("" :: "v"(v0), "v"(v1), "v"(v2), "v"(v3), "s"(kvbase), "s"(lds0));
  asm volatile("" :: "s"(q), "s"(kc), "s"(vc), "s"(ws));
  if (__builtin_expect(wave < NWAVE, 1)) {
    const __amdgpu_buffer_rsrc_t rk = __builtin_amdgcn_make_buffer_rsrc((void*)kc, 0, (int)KV_BYTES, 0x00020000);
    {
#pragma unroll
      for (int t = 0; t < 4; ++t)
        asm volatile("s_mov_b32 m0, %0\n\ts_nop 0\n\t"
                     "buffer_load_dwordx4 %1, %5, %6 offen" K_POL " lds\n\t"
                     "buffer_load_dwordx4 %2, %5, %6 offen offset:1024" K_POL " lds\n\t"
                     "buffer_load_dwordx4 %3, %5, %6 offen offset:2048" K_POL " lds\n\t"
                     "buffer_load_dwordx4 %4, %5, %6 offen offset:3072" K_POL " lds"
                     :: "s"(lds0 + t * 4096), "v"(v0), "v"(v1), "v"(v2), "v"(v3), "s"(rk), "s"(kvbase + t * 4 * PSTR) : "m0", "memory");
    }
    u32x4 Q[4];
    {
      const unsigned short* qp = q + (size_t)(group * REP + (c & 7)) * D + g * 8;
#pragma unroll
      for (int s = 0; s < 4; ++s) Q[s] = *reinterpret_cast<const u32x4*>(qp + s * 32);
    }
    __builtin_amdgcn_sched_barrier(0);
    const __amdgpu_buffer_rsrc_t rv = __builtin_amdgcn_make_buffer_rsrc((void*)vc, 0, (int)KV_BYTES, 0x00020000);
    u32x4 V[16];
#pragma unroll
    for (int n = 0; n < 16; ++n) V[n] = __builtin_amdgcn_raw_buffer_load_b128(rv, lane * 16, kvbase + n * PSTR, V_NT ? 2 : 0);
    __builtin_amdgcn_sched_barrier(0);
    asm volatile("s_waitcnt vmcnt(16)" ::: "memory");
    __builtin_amdgcn_sched_barrier(0);
    f32x4 S[4];
    {
      const int row = 4 * (c & 3) + (c >> 2);
      const unsigned char* kb = smem + wave * WREG + row * 256;
#pragma unroll
      for (int t = 0; t < 4; ++t) {
        f32x4 a = {0.f, 0.f, 0.f, 0.f};
#pragma unroll
        for (int s = 0; s < 4; ++s) {
          const u32x4 kf = *reinterpret_cast<const u32x4*>(kb + t * 4096 + (((4 * s + g) ^ c) << 4));
          a = mfma16(kf, Q[s], a);
        }
        S[t] = a;
      }
    }
    float m = S[0][0];
#pragma unroll
    for (int t = 0; t < 4; ++t)
#pragma unroll
      for (int r = 0; r < 4; ++r) m = fmaxf(m, S[t][r]);
    m = xrow16_max(m);
    const float mc = m * C2;
    float l = 0.f;
    u32x4 P[2];
#pragma unroll
    for (int u = 0; u < 2; ++u) {
      float p[8];
#pragma unroll
      for (int j = 0; j < 8; ++j) { p[j] = __builtin_amdgcn_exp2f(fmaf(S[2 * u + (j >> 2)][j & 3], C2, -mc)); l += p[j]; }
#pragma unroll
      for (int i = 0; i < 4; ++i) P[u][i] = cvtpk(p[2 * i], p[2 * i + 1]);
    }
    l = xrow16_sum(l);
    if (lane < 8) { stat[lane * NWAVE + wave] = mc; stat[64 + lane * NWAVE + wave] = l; }
    __builtin_amdgcn_sched_barrier(0);
    f32x4 acc[8];
#pragma unroll
    for (int u = 0; u < 2; ++u) {
#pragma unroll
      for (int e = 0; e < 8; ++e) {
        u32x4 A;
#pragma unroll
        for (int i = 0; i < 4; ++i) A[i] = __builtin_amdgcn_perm(V[8 * u + 2 * i + 1][e >> 1], V[8 * u + 2 * i][e >> 1], (e & 1) ? 0x07060302u : 0x05040100u);
        acc[e] = mfma16(A, P[u], u == 0 ? f32x4{0.f, 0.f, 0.f, 0.f} : acc[e]);
      }
#pragma unroll
      for (int e = 0; e < 8; ++e) { __builtin_amdgcn_sched_group_barrier(0x2, 4, 0); __builtin_amdgcn_sched_group_barrier(0x8, 1, 0); }
      __builtin_amdgcn_sched_barrier(0);
    }
    if (c < 8) {
      float* o = reinterpret_cast<float*>(smem + wave * WREG) + c * OSTR + 4 * g;
#pragma unroll
      for (int e = 0; e < 8; ++e) *reinterpret_cast<f32x4*>(o + 16 * e) = acc[e];
    }
    asm volatile("s_waitcnt lgkmcnt(0)\n\ts_barrier" ::: "memory");
    {
      const int h = wave;
      const __amdgpu_buffer_rsrc_t rw = __builtin_amdgcn_make_buffer_rsrc((void*)(ws + (size_t)group * WS_GROUP), 0, (int)WS_GROUP, 0x00020000);
      f32x2 ov[NWAVE];
#pragma unroll
      for (int w = 0; w < NWAVE; ++w) ov[w] = *reinterpret_cast<const f32x2*>(smem + w * WREG + (h * OSTR + 2 * lane) * 4);
      const f32x4 l0 = *reinterpret_cast<const f32x4*>(stat + 64 + h * NWAVE), l1 = *reinterpret_cast<const f32x4*>(stat + 64 + h * NWAVE + 4);
      const unsigned tag = reinterpret_cast<const unsigned*>(stat)[128];
      const f32x4 m0 = *reinterpret_cast<const f32x4*>(stat + h * NWAVE), m1 = *reinterpret_cast<const f32x4*>(stat + h * NWAVE + 4);
      const float Mh = fmaxf(fmaxf(fmaxf(m0[0], m0[1]), fmaxf(m0[2], m0[3])), fmaxf(fmaxf(m1[0], m1[1]), fmaxf(m1[2], m1[3])));
      float f[NWAVE];
#pragma unroll
      for (int w = 0; w < 4; ++w) { f[w] = __builtin_amdgcn_exp2f(m0[w] - Mh); f[4 + w] = __builtin_amdgcn_exp2f(m1[w] - Mh); }
      const float L = (fmaf(f[1], l0[1], f[0] * l0[0]) + fmaf(f[3], l0[3], f[2] * l0[2])) + (fmaf(f[5], l1[1], f[4] * l1[0]) + fmaf(f[7], l1[3], f[6] * l1[2]));
      const float inv = __builtin_amdgcn_rcpf(L);
      const float o0 = ((fmaf(f[1], ov[1][0], f[0] * ov[0][0]) + fmaf(f[3], ov[3][0], f[2] * ov[2][0])) + (fmaf(f[5], ov[5][0], f[4] * ov[4][0]) + fmaf(f[7], ov[7][0], f[6] * ov[6][0]))) * inv;
      const float o1 = ((fmaf(f[1], ov[1][1], f[0] * ov[0][1]) + fmaf(f[3], ov[3][1], f[2] * ov[2][1])) + (fmaf(f[5], ov[5][1], f[4] * ov[4][1]) + fmaf(f[7], ov[7][1], f[6] * ov[6][1]))) * inv;
      const unsigned lse = __float_as_uint(Mh + __builtin_amdgcn_logf(L));
      const unsigned a = (__float_as_uint(o0) + 0x200u) & 0xfffffc00u, b = (__float_as_uint(o1) + 0x200u) & 0xfffffc00u;
      const unsigned hi = a | (b >> 22), lo = (b << 10) | (((lse >> (2 * c)) & 3u) << 18) | tag;
      __builtin_amdgcn_raw_buffer_store_b64(u32x2{lo, hi}, rw, WS_REC + ((4 * h + g) * NSPLIT + split) * 128 + c * 8, 0, 16);
    }
    return;
  }

  {
    const __amdgpu_buffer_rsrc_t rw = __builtin_amdgcn_make_buffer_rsrc((void*)(ws + (size_t)group * WS_GROUP), 0, (int)WS_GROUP, 0x00020000);
    const int ch = split >> 2, cq = split & 3, p = lane & 7, rec = lane >> 3;
    const unsigned long long gen = __hip_atomic_load((GAS unsigned long long*)(ws + (size_t)group * WS_GROUP), __ATOMIC_RELAXED, __HIP_MEMORY_SCOPE_AGENT);
    const unsigned tq = *reinterpret_cast<const unsigned*>(q + (size_t)(group * REP + ch) * D + 2 * lane);
    const unsigned tk = *reinterpret_cast<const unsigned*>(kt + (size_t)group * D + 2 * lane);
    const int dout = 32 * (p & 3) + 8 * (rec & 3) + 2 * cq + (p >> 2);
    const unsigned tv = vt[(size_t)group * D + dout];
    const unsigned tmask = tm[group >> 2];
    GAS unsigned short* outp = (GAS unsigned short*)out + (size_t)(group * REP + ch) * D + dout;
    {
      u32x4 x[4];
#pragma unroll
      for (int k = 0; k < 4; ++k) x[k] = __builtin_amdgcn_raw_buffer_load_b128(rw, WS_REC + split * (NSPLIT * 128) + lane * 16 + k * 1024, 0, 16);
      const unsigned y = __hip_atomic_load(outp, __ATOMIC_RELAXED, __HIP_MEMORY_SCOPE_WORKGROUP);
      asm volatile("" :: "v"(x[0]), "v"(x[1]), "v"(x[2]), "v"(x[3]), "v"(y));
    }
    float st = bflo(tq) * bflo(tk) + bfhi(tq) * bfhi(tk);
    st = xrow16_sum(row16_sum(st));
    float mt = st * C2 + (1.0f - bflo(tmask)) * (-1.0e9f * 1.4426950408889634f);
    float tvf = bflo(tv);
    const unsigned tag = __builtin_amdgcn_readfirstlane(((unsigned)gen + 1u) & TAGM);
    asm volatile("" : "+v"(tvf), "+v"(mt));
    if (lane == 0) reinterpret_cast<unsigned*>(stat)[128] = tag;
    asm volatile("s_waitcnt lgkmcnt(0)\n\ts_barrier" ::: "memory");
    STAMP(4);
    __builtin_amdgcn_s_sleep(POLL_FIRST);
    u32x4 o[4];
    bool good = false;
    for (unsigned spin = 0; spin < SPIN_CAP; ++spin) {
      asm volatile("" ::: "memory");
#pragma unroll
      for (int k = 0; k < 4; ++k) o[k] = __builtin_amdgcn_raw_buffer_load_b128(rw, WS_REC + split * (NSPLIT * 128) + lane * 16 + k * 1024, 0, 16);
      unsigned bad = 0;
#pragma unroll
      for (int k = 0; k < 4; ++k) bad |= (o[k][0] ^ tag) | (o[k][2] ^ tag);
      if (__all((bad & TAGM) == 0)) { good = true; break; }
      __builtin_amdgcn_s_sleep(POLL_SLEEP);
    }
    float lse[4], M = mt;
#pragma unroll
    for (int k = 0; k < 4; ++k) {
      unsigned x = (((o[k][0] >> 18) & 3u) | ((o[k][2] >> 16) & 12u)) << (4 * p);
      x |= dppu<XOR1>(x); x |= dppu<XOR2>(x); x |= dppu<HMIR>(x);
      lse[k] = __uint_as_float(x);
      M = fmaxf(M, lse[k]);
    }
    M = fmaxf(M, dppf<ROR8>(M));
    M = xrow16_max(M);
    float L = 0.f, a0 = 0.f, a1 = 0.f, a2 = 0.f, a3 = 0.f;
#pragma unroll
    for (int k = 0; k < 4; ++k) {
      const float w = __builtin_amdgcn_exp2f(lse[k] - M);
      L += w;
      a0 = fmaf(w, __uint_as_float(o[k][1] & 0xfffffc00u), a0);
      a1 = fmaf(w, __uint_as_float(__builtin_amdgcn_alignbit(o[k][1], o[k][0], 10) & 0xfffffc00u), a1);
      a2 = fmaf(w, __uint_as_float(o[k][3] & 0xfffffc00u), a2);
      a3 = fmaf(w, __uint_as_float(__builtin_amdgcn_alignbit(o[k][3], o[k][2], 10) & 0xfffffc00u), a3);
    }
    L += dppf<ROR8>(L); a0 += dppf<ROR8>(a0); a1 += dppf<ROR8>(a1); a2 += dppf<ROR8>(a2); a3 += dppf<ROR8>(a3);
    L = xrow16_sum(L); a0 = xrow16_sum(a0); a1 = xrow16_sum(a1); a2 = xrow16_sum(a2); a3 = xrow16_sum(a3);
    const float wt = __builtin_amdgcn_exp2f(mt - M);
    const float sel = (rec & 2) ? ((rec & 1) ? a3 : a2) : ((rec & 1) ? a1 : a0);
    const float inv = good ? __builtin_amdgcn_rcpf(L + wt) : __builtin_nanf("");
    const float res = fmaf(wt, tvf, sel) * inv;
    if (rec < 4) {
      __hip_atomic_store(outp, (unsigned short)cvtpk(res, res), __ATOMIC_RELAXED, __HIP_MEMORY_SCOPE_AGENT);
    }
    if (split == 0 && lane == 0 && good)
      __hip_atomic_store((GAS unsigned long long*)(ws + (size_t)group * WS_GROUP), gen + 1ull, __ATOMIC_RELAXED, __HIP_MEMORY_SCOPE_AGENT);
    STAMP(6);
  }
}

extern "C" void kernel_launch(void* const* d_in, const int* in_sizes, int n_in, void* d_out, int out_size, void* d_ws, size_t ws_size,
                              hipStream_t stream) {
  tree_attn_decode<<<256, 576, 0, stream>>>((const unsigned short*)d_in[0], (const unsigned short*)d_in[1], (const unsigned short*)d_in[2],
                                            (const unsigned short*)d_in[3], (const unsigned short*)d_in[4], (const unsigned short*)d_in[5],
                                            (unsigned short*)d_out, (unsigned char*)d_ws + (size_t)(WS_OFF) * (1u << 20));
}
```

```cpp
#include <hip/hip_runtime.h>
#include <stdint.h>

typedef __attribute__((ext_vector_type(4))) unsigned u32x4;
typedef __attribute__((ext_vector_type(2))) unsigned u32x2;
typedef __attribute__((ext_vector_type(8))) __bf16 bf16x8;
typedef __attribute__((ext_vector_type(4))) float f32x4;
typedef __attribute__((ext_vector_type(2))) float f32x2;
#define LAS __attribute__((address_space(3)))
#define GAS __attribute__((address_space(1)))

#define K_NT 1
#define V_NT 1
#define ORD 1
#define OUT_POL 0
#define POLL_FIRST 24
#define POLL_SLEEP 4
#define WS_OFF 0
#define K_POL " nt"

constexpr int D = 128, CTX = 16384, REP = 8;
constexpr int NSPLIT = 32, KEYS_WG = 512, NWAVE = 8;
constexpr float C2 = 0.088388347648318440f * 1.4426950408889634f;
constexpr unsigned KV_BYTES = 2u * 4u * CTX * D * 2u;
constexpr int WREG = 16384;
constexpr int OSTR = 132;
constexpr int LDS_STAT = NWAVE * WREG;
constexpr unsigned PSTR = ORD ? 8192u : 1024u, WSTR = ORD ? 1024u : 16384u;
constexpr unsigned WS_GROUP = 256u * 1024u, WS_REC = 4096u;
constexpr unsigned SPIN_CAP = 1u << 18;
constexpr unsigned TAGM = 0x3ffffu;

__device__ __forceinline__ f32x4 mfma16(u32x4 a, u32x4 b, f32x4 c) {
  return __builtin_amdgcn_mfma_f32_16x16x32_bf16(__builtin_bit_cast(bf16x8, a), __builtin_bit_cast(bf16x8, b), c, 0, 0, 0);
}
__device__ __forceinline__ unsigned cvtpk(float lo, float hi) {
  typedef __attribute__((ext_vector_type(2))) __bf16 bf16x2;
  bf16x2 r = {(__bf16)lo, (__bf16)hi};
  return __builtin_bit_cast(unsigned, r);
}
__device__ __forceinline__ float max2f(float a, float b) { return __builtin_amdgcn_fmed3f(a, b, 3.4028234e38f); }
__device__ __forceinline__ float max3f(float a, float b, float c) { return fmaxf(fmaxf(a, b), c); }
__device__ __forceinline__ float xrow16_max(float x) {
  auto s = __builtin_amdgcn_permlane16_swap(__float_as_uint(x), __float_as_uint(x), false, false);
  x = max2f(__uint_as_float(s[0]), __uint_as_float(s[1]));
  auto t = __builtin_amdgcn_permlane32_swap(__float_as_uint(x), __float_as_uint(x), false, false);
  return max2f(__uint_as_float(t[0]), __uint_as_float(t[1]));
}
__device__ __forceinline__ float xrow16_sum(float x) {
  auto s = __builtin_amdgcn_permlane16_swap(__float_as_uint(x), __float_as_uint(x), false, false);
  x = __uint_as_float(s[0]) + __uint_as_float(s[1]);
  auto t = __builtin_amdgcn_permlane32_swap(__float_as_uint(x), __float_as_uint(x), false, false);
  return __uint_as_float(t[0]) + __uint_as_float(t[1]);
}
template <int CTRL> __device__ __forceinline__ float dppf(float x) {
  return __builtin_bit_cast(float, __builtin_amdgcn_mov_dpp(__builtin_bit_cast(int, x), CTRL, 0xf, 0xf, true));
}
template <int CTRL> __device__ __forceinline__ unsigned dppu(unsigned x) { return (unsigned)__builtin_amdgcn_mov_dpp((int)x, CTRL, 0xf, 0xf, true); }
constexpr int XOR1 = 0xB1, XOR2 = 0x4E, HMIR = 0x141, ROR8 = 0x128;
__device__ __forceinline__ float row16_sum(float x) { x += dppf<XOR1>(x); x += dppf<XOR2>(x); x += dppf<HMIR>(x); x += dppf<ROR8>(x); return x; }
__device__ __forceinline__ float bflo(unsigned u) { return __uint_as_float(u << 16); }
__device__ __forceinline__ float bfhi(unsigned u) { return __uint_as_float(u & 0xffff0000u); }

__global__ void __launch_bounds__(576, 3) tree_attn_decode(const unsigned short* __restrict__ q, const unsigned short* __restrict__ kc,
                                                           const unsigned short* __restrict__ vc, const unsigned short* __restrict__ kt,
                                                           const unsigned short* __restrict__ vt, const unsigned short* __restrict__ tm,
                                                           unsigned short* __restrict__ out, unsigned char* __restrict__ ws) {
  __shared__ __attribute__((aligned(16))) unsigned char smem[NWAVE * WREG + 640];
  const int bid = blockIdx.x, group = bid & 7, split = bid >> 3;
  const int tid = threadIdx.x, lane = tid & 63, wave = __builtin_amdgcn_readfirstlane(tid >> 6), g = lane >> 4, c = lane & 15;
  float* stat = reinterpret_cast<float*>(smem + LDS_STAT);

  constexpr unsigned X = PSTR - 1024u;
  const unsigned v0 = g * 256 + ((c ^ (4 * g + 0)) << 4), v1 = X + g * 256 + ((c ^ (4 * g + 1)) << 4), v2 = 2 * X + g * 256 + ((c ^ (4 * g + 2)) << 4), v3 = 3 * X + g * 256 + ((c ^ (4 * g + 3)) << 4);
  const unsigned kvbase = (unsigned)(group * CTX + split * KEYS_WG) * (D * 2) + (unsigned)wave * WSTR;
  const unsigned lds0 = (unsigned)(uintptr_t)(LAS unsigned char*)smem + (unsigned)wave * WREG;
  asm volatile("" :: "v"(v0), "v"(v1), "v"(v2), "v"(v3), "s"(kvbase), "s"(lds0));
  asm volatile("" :: "s"(q), "s"(kc), "s"(vc), "s"(ws));
  if (__builtin_expect(wave < NWAVE, 1)) {
    const __amdgpu_buffer_rsrc_t rk = __builtin_amdgcn_make_buffer_rsrc((void*)kc, 0, (int)KV_BYTES, 0x00020000);
    {
#pragma unroll
      for (int t = 0; t < 4; ++t)
        asm volatile("s_mov_b32 m0, %0\n\ts_nop 0\n\t"
                     "buffer_load_dwordx4 %1, %5, %6 offen" K_POL " lds\n\t"
                     "buffer_load_dwordx4 %2, %5, %6 offen offset:1024" K_POL " lds\n\t"
                     "buffer_load_dwordx4 %3, %5, %6 offen offset:2048" K_POL " lds\n\t"
                     "buffer_load_dwordx4 %4, %5, %6 offen offset:3072" K_POL " lds"
                     :: "s"(lds0 + t * 4096), "v"(v0), "v"(v1), "v"(v2), "v"(v3), "s"(rk), "s"(kvbase + t * 4 * PSTR) : "m0", "memory");
    }
    u32x4 Q[4];
    {
      const unsigned short* qp = q + (size_t)(group * REP + (c & 7)) * D + g * 8;
#pragma unroll
      for (int s = 0; s < 4; ++s) Q[s] = *reinterpret_cast<const u32x4*>(qp + s * 32);
    }
    __builtin_amdgcn_sched_barrier(0);
    const __amdgpu_buffer_rsrc_t rv = __builtin_amdgcn_make_buffer_rsrc((void*)vc, 0, (int)KV_BYTES, 0x00020000);
    u32x4 V[16];
#pragma unroll
    for (int n = 0; n < 16; ++n) V[n] = __builtin_amdgcn_raw_buffer_load_b128(rv, lane * 16, kvbase + n * PSTR, V_NT ? 2 : 0);
    __builtin_amdgcn_sched_barrier(0);
    asm volatile("s_waitcnt vmcnt(16)" ::: "memory");
    __builtin_amdgcn_sched_barrier(0);
    f32x4 S[4];
    {
      const int row = 4 * (c & 3) + (c >> 2);
      const unsigned char* kb = smem + wave * WREG + row * 256;
#pragma unroll
      for (int t = 0; t < 4; ++t) {
        f32x4 a = {0.f, 0.f, 0.f, 0.f};
#pragma unroll
        for (int s = 0; s < 4; ++s) {
          const u32x4 kf = *reinterpret_cast<const u32x4*>(kb + t * 4096 + (((4 * s + g) ^ c) << 4));
          a = mfma16(kf, Q[s], a);
        }
        S[t] = a;
      }
    }
    float m = S[0][0];
#pragma unroll
    for (int t = 0; t < 4; ++t)
#pragma unroll
      for (int r = 0; r < 4; ++r) m = fmaxf(m, S[t][r]);
    m = xrow16_max(m);
    const float mc = m * C2;
    float l = 0.f;
    u32x4 P[2];
#pragma unroll
    for (int u = 0; u < 2; ++u) {
      float p[8];
#pragma unroll
      for (int j = 0; j < 8; ++j) { p[j] = __builtin_amdgcn_exp2f(fmaf(S[2 * u + (j >> 2)][j & 3], C2, -mc)); l += p[j]; }
#pragma unroll
      for (int i = 0; i < 4; ++i) P[u][i] = cvtpk(p[2 * i], p[2 * i + 1]);
    }
    l = xrow16_sum(l);
    if (lane < 8) { stat[lane * NWAVE + wave] = mc; stat[64 + lane * NWAVE + wave] = l; }
    __builtin_amdgcn_sched_barrier(0);
    const int h = wave;
    const __amdgpu_buffer_rsrc_t rw = __builtin_amdgcn_make_buffer_rsrc((void*)(ws + (size_t)group * WS_GROUP), 0, (int)WS_GROUP, 0x00020000);
    LAS unsigned char* lo_ = (LAS unsigned char*)smem;
    unsigned roff = WS_REC + ((4 * h + g) * NSPLIT + split) * 128 + c * 8;
    unsigned ao = (h * OSTR + 2 * lane) * 4, ao2 = ao + 4 * WREG, as = LDS_STAT + h * (NWAVE * 4), sh = 2 * c, aw = wave * WREG + (c * OSTR + 4 * g) * 4;
    asm volatile("" : "+v"(roff), "+v"(ao), "+v"(ao2), "+v"(as), "+v"(sh), "+v"(aw) : "s"(rw));
    __builtin_amdgcn_sched_barrier(0);
    f32x4 acc[8];
#pragma unroll
    for (int u = 0; u < 2; ++u) {
#pragma unroll
      for (int e = 0; e < 8; ++e) {
        u32x4 A;
#pragma unroll
        for (int i = 0; i < 4; ++i) A[i] = __builtin_amdgcn_perm(V[8 * u + 2 * i + 1][e >> 1], V[8 * u + 2 * i][e >> 1], (e & 1) ? 0x07060302u : 0x05040100u);
        acc[e] = mfma16(A, P[u], u == 0 ? f32x4{0.f, 0.f, 0.f, 0.f} : acc[e]);
      }
#pragma unroll
      for (int e = 0; e < 1; ++e) { __builtin_amdgcn_sched_group_barrier(0x2, 8, 0); __builtin_amdgcn_sched_group_barrier(0x8, 1, 0); }
#pragma unroll
      for (int e = 1; e < 8; ++e) { __builtin_amdgcn_sched_group_barrier(0x2, 4, 0); __builtin_amdgcn_sched_group_barrier(0x8, 1, 0); }
      __builtin_amdgcn_sched_barrier(0);
    }
    if (c < 8) {
#pragma unroll
      for (int e = 0; e < 8; ++e) *(LAS f32x4*)(lo_ + aw + 64 * e) = acc[e];
    }
    asm volatile("s_waitcnt lgkmcnt(0)\n\ts_barrier" ::: "memory");
    {
      const f32x4 m0 = *(const LAS f32x4*)(lo_ + as), m1 = *(const LAS f32x4*)(lo_ + as + 16);
      const f32x4 l0 = *(const LAS f32x4*)(lo_ + as + 256), l1 = *(const LAS f32x4*)(lo_ + as + 272);
      const unsigned tag = *(const LAS unsigned*)(lo_ + LDS_STAT + 512);
      __builtin_amdgcn_sched_barrier(0);
      f32x2 ov[NWAVE];
#pragma unroll
      for (int w = 0; w < NWAVE; ++w) ov[w] = *(const LAS f32x2*)(lo_ + (w < 4 ? ao : ao2) + (w & 3) * WREG);
      __builtin_amdgcn_sched_barrier(0);
      const float Mh = max3f(max3f(m0[0], m0[1], m0[2]), max3f(m0[3], m1[0], m1[1]), max2f(m1[2], m1[3]));
      float f[NWAVE];
#pragma unroll
      for (int w = 0; w < 4; ++w) { f[w] = __builtin_amdgcn_exp2f(m0[w] - Mh); f[4 + w] = __builtin_amdgcn_exp2f(m1[w] - Mh); }
      const float L = (fmaf(f[1], l0[1], f[0] * l0[0]) + fmaf(f[3], l0[3], f[2] * l0[2])) + (fmaf(f[5], l1[1], f[4] * l1[0]) + fmaf(f[7], l1[3], f[6] * l1[2]));
      const float inv = __builtin_amdgcn_rcpf(L);
      const unsigned lse = __float_as_uint(Mh + __builtin_amdgcn_logf(L));
      const unsigned lt = (((lse >> sh) & 3u) << 18) | tag;
      const f32x2 ot = ((f[0] * ov[0] + f[1] * ov[1]) + (f[2] * ov[2] + f[3] * ov[3])) + ((f[4] * ov[4] + f[5] * ov[5]) + (f[6] * ov[6] + f[7] * ov[7]));
      const unsigned a = (__float_as_uint(ot[0] * inv) + 0x200u) & 0xfffffc00u, b = __float_as_uint(ot[1] * inv) + 0x200u;
      const unsigned hi = a | (b >> 22), lo = ((b << 10) & 0xfff00000u) | lt;
      __builtin_amdgcn_raw_buffer_store_b64(u32x2{lo, hi}, rw, roff, 0, 16);
    }
    return;
  }

  {
    const __amdgpu_buffer_rsrc_t rw = __builtin_amdgcn_make_buffer_rsrc((void*)(ws + (size_t)group * WS_GROUP), 0, (int)WS_GROUP, 0x00020000);
    const int ch = split >> 2, cq = split & 3, p = lane & 7;
    const unsigned long long gen = __hip_atomic_load((GAS unsigned long long*)(ws + (size_t)group * WS_GROUP), __ATOMIC_RELAXED, __HIP_MEMORY_SCOPE_AGENT);
    const unsigned tq = *reinterpret_cast<const unsigned*>(q + (size_t)(group * REP + ch) * D + 2 * lane);
    const unsigned tk = *reinterpret_cast<const unsigned*>(kt + (size_t)group * D + 2 * lane);
    const int dout = 32 * (p & 3) + 8 * g + 2 * cq + (p >> 2);
    const unsigned tv = vt[(size_t)group * D + dout];
    const unsigned tmask = tm[group >> 2];
    GAS unsigned short* outp = (GAS unsigned short*)out + (size_t)(group * REP + ch) * D + dout;
    {
      u32x4 x[4];
#pragma unroll
      for (int k = 0; k < 4; ++k) x[k] = __builtin_amdgcn_raw_buffer_load_b128(rw, WS_REC + split * (NSPLIT * 128) + lane * 16 + k * 1024, 0, 16);
      const unsigned y = __hip_atomic_load(outp, __ATOMIC_RELAXED, __HIP_MEMORY_SCOPE_WORKGROUP);
      asm volatile("" :: "v"(x[0]), "v"(x[1]), "v"(x[2]), "v"(x[3]), "v"(y));
    }
    float st = bflo(tq) * bflo(tk) + bfhi(tq) * bfhi(tk);
    st = xrow16_sum(row16_sum(st));
    float mt = st * C2 + (1.0f - bflo(tmask)) * (-1.0e9f * 1.4426950408889634f);
    float tvf = bflo(tv);
    const unsigned tag = __builtin_amdgcn_readfirstlane(((unsigned)gen + 1u) & TAGM);
    asm volatile("" : "+v"(tvf), "+v"(mt));
    if (lane == 0) reinterpret_cast<unsigned*>(stat)[128] = tag;
    asm volatile("s_waitcnt lgkmcnt(0)\n\ts_barrier" ::: "memory");
    __builtin_amdgcn_s_sleep(POLL_FIRST);
    u32x4 o[4];
    bool good = false;
    for (unsigned spin = 0; spin < SPIN_CAP; ++spin) {
      asm volatile("" ::: "memory");
#pragma unroll
      for (int k = 0; k < 4; ++k) o[k] = __builtin_amdgcn_raw_buffer_load_b128(rw, WS_REC + split * (NSPLIT * 128) + lane * 16 + k * 1024, 0, 16);
      unsigned bad = 0;
#pragma unroll
      for (int k = 0; k < 4; ++k) bad |= (o[k][0] ^ tag) | (o[k][2] ^ tag);
      if (__all((bad & TAGM) == 0)) { good = true; break; }
      __builtin_amdgcn_s_sleep(POLL_SLEEP);
    }
    float lse[4], M = mt;
#pragma unroll
    for (int k = 0; k < 4; ++k) {
      unsigned x = (((o[k][0] >> 18) & 3u) | ((o[k][2] >> 16) & 12u)) << (4 * p);
      x |= dppu<XOR1>(x); x |= dppu<XOR2>(x); x |= dppu<HMIR>(x);
      lse[k] = __uint_as_float(x);
      M = max2f(M, lse[k]);
    }
    M = max2f(M, dppf<ROR8>(M));
    { auto s1 = __builtin_amdgcn_permlane16_swap(__float_as_uint(M), __float_as_uint(M), false, false); M = max2f(__uint_as_float(s1[0]), __uint_as_float(s1[1]));
      auto s2 = __builtin_amdgcn_permlane32_swap(__float_as_uint(M), __float_as_uint(M), false, false); M = max2f(__uint_as_float(s2[0]), __uint_as_float(s2[1])); }
    float L = 0.f, a0 = 0.f, a1 = 0.f, a2 = 0.f, a3 = 0.f;
#pragma unroll
    for (int k = 0; k < 4; ++k) {
      const float w = __builtin_amdgcn_exp2f(lse[k] - M);
      L += w;
      a0 = fmaf(w, __uint_as_float(o[k][1] & 0xfffffc00u), a0);
      a1 = fmaf(w, __uint_as_float(__builtin_amdgcn_alignbit(o[k][1], o[k][0], 10) & 0xfffffc00u), a1);
      a2 = fmaf(w, __uint_as_float(o[k][3] & 0xfffffc00u), a2);
      a3 = fmaf(w, __uint_as_float(__builtin_amdgcn_alignbit(o[k][3], o[k][2], 10) & 0xfffffc00u), a3);
    }
    const float wt = __builtin_amdgcn_exp2f(mt - M);
    auto t01 = __builtin_amdgcn_permlane16_swap(__float_as_uint(a0), __float_as_uint(a1), false, false);
    auto t23 = __builtin_amdgcn_permlane16_swap(__float_as_uint(a2), __float_as_uint(a3), false, false);
    const float b0 = __uint_as_float(t01[0]) + __uint_as_float(t01[1]), b1 = __uint_as_float(t23[0]) + __uint_as_float(t23[1]);
    auto tb = __builtin_amdgcn_permlane32_swap(__float_as_uint(b0), __float_as_uint(b1), false, false);
    float sel = __uint_as_float(tb[0]) + __uint_as_float(tb[1]);
    sel += dppf<ROR8>(sel);
    L += dppf<ROR8>(L);
    L = xrow16_sum(L);
    const float inv = good ? __builtin_amdgcn_rcpf(L + wt) : __builtin_nanf("");
    const float res = fmaf(wt, tvf, sel) * inv;
    if ((lane & 8) == 0) {
      *outp = (unsigned short)cvtpk(res, res);
    }
    if (split == 0 && lane == 0 && good)
      __hip_atomic_store((GAS unsigned long long*)(ws + (size_t)group * WS_GROUP), gen + 1ull, __ATOMIC_RELAXED, __HIP_MEMORY_SCOPE_AGENT);
  }
}

extern "C" void kernel_launch(void* const* d_in, const int* in_sizes, int n_in, void* d_out, int out_size, void* d_ws, size_t ws_size,
                              hipStream_t stream) {
  tree_attn_decode<<<256, 576, 0, stream>>>((const unsigned short*)d_in[0], (const unsigned short*)d_in[1], (const unsigned short*)d_in[2],
                                            (const unsigned short*)d_in[3], (const unsigned short*)d_in[4], (const unsigned short*)d_in[5],
                                            (unsigned short*)d_out, (unsigned char*)d_ws + (size_t)(WS_OFF) * (1u << 20));
}
```

```cpp
#include <hip/hip_runtime.h>
#include <stdint.h>

typedef __attribute__((ext_vector_type(4))) unsigned u32x4;
typedef __attribute__((ext_vector_type(2))) unsigned u32x2;
typedef __attribute__((ext_vector_type(8))) __bf16 bf16x8;
typedef __attribute__((ext_vector_type(4))) float f32x4;
typedef __attribute__((ext_vector_type(2))) float f32x2;
#define LAS __attribute__((address_space(3)))
#define GAS __attribute__((address_space(1)))

constexpr int D = 128, CTX = 16384, REP = 8;
constexpr int NSPLIT = 32, KEYS_WG = 512, NWAVE = 8;
constexpr float C2 = 0.088388347648318440f * 1.4426950408889634f;
constexpr float NEG2 = -1.0e9f * 1.4426950408889634f;
constexpr unsigned KV_BYTES = 2u * 4u * CTX * D * 2u;
constexpr int WREG = 16384;
constexpr int PREG = WREG + 32;
constexpr int OSTR = 132;
constexpr int LDS_STAT = NWAVE * WREG;
constexpr unsigned PSTR = 8192u, WSTR = 1024u;
constexpr unsigned WS_GROUP = 256u * 1024u, WS_REC = 4096u;
constexpr unsigned TAGM = 0x3ffffu;
constexpr unsigned SPIN_CAP = 1u << 18;
constexpr int POLL_FIRST = 24, POLL_SLEEP = 4;

__device__ __forceinline__ f32x4 mfma16(u32x4 a, u32x4 b, f32x4 c) {
  return __builtin_amdgcn_mfma_f32_16x16x32_bf16(__builtin_bit_cast(bf16x8, a), __builtin_bit_cast(bf16x8, b), c, 0, 0, 0);
}
__device__ __forceinline__ unsigned cvtpk(float lo, float hi) {
  typedef __attribute__((ext_vector_type(2))) __bf16 bf16x2;
  bf16x2 r = {(__bf16)lo, (__bf16)hi};
  return __builtin_bit_cast(unsigned, r);
}
__device__ __forceinline__ float max2f(float a, float b) { return __builtin_amdgcn_fmed3f(a, b, 3.4028234e38f); }
__device__ __forceinline__ float max3f(float a, float b, float c) { return fmaxf(fmaxf(a, b), c); }
__device__ __forceinline__ float xrow16_max(float x) {
  auto s = __builtin_amdgcn_permlane16_swap(__float_as_uint(x), __float_as_uint(x), false, false);
  x = max2f(__uint_as_float(s[0]), __uint_as_float(s[1]));
  auto t = __builtin_amdgcn_permlane32_swap(__float_as_uint(x), __float_as_uint(x), false, false);
  return max2f(__uint_as_float(t[0]), __uint_as_float(t[1]));
}
__device__ __forceinline__ float xrow16_sum(float x) {
  auto s = __builtin_amdgcn_permlane16_swap(__float_as_uint(x), __float_as_uint(x), false, false);
  x = __uint_as_float(s[0]) + __uint_as_float(s[1]);
  auto t = __builtin_amdgcn_permlane32_swap(__float_as_uint(x), __float_as_uint(x), false, false);
  return __uint_as_float(t[0]) + __uint_as_float(t[1]);
}
template <int CTRL> __device__ __forceinline__ float dppf(float x) {
  return __builtin_bit_cast(float, __builtin_amdgcn_mov_dpp(__builtin_bit_cast(int, x), CTRL, 0xf, 0xf, true));
}
template <int CTRL> __device__ __forceinline__ unsigned dppu(unsigned x) { return (unsigned)__builtin_amdgcn_mov_dpp((int)x, CTRL, 0xf, 0xf, true); }
constexpr int XOR1 = 0xB1, XOR2 = 0x4E, HMIR = 0x141, ROR8 = 0x128;
__device__ __forceinline__ float row16_sum(float x) { x += dppf<XOR1>(x); x += dppf<XOR2>(x); x += dppf<HMIR>(x); x += dppf<ROR8>(x); return x; }
__device__ __forceinline__ float bflo(unsigned u) { return __uint_as_float(u << 16); }
__device__ __forceinline__ float bfhi(unsigned u) { return __uint_as_float(u & 0xffff0000u); }

__global__ void __launch_bounds__(576, 3) tree_attn_decode(const unsigned short* __restrict__ q, const unsigned short* __restrict__ kc,
                                                           const unsigned short* __restrict__ vc, const unsigned short* __restrict__ kt,
                                                           const unsigned short* __restrict__ vt, const unsigned short* __restrict__ tm,
                                                           unsigned short* __restrict__ out, unsigned char* __restrict__ ws) {
  __shared__ __attribute__((aligned(16))) unsigned char smem[NWAVE * WREG + 640];
  const int bid = blockIdx.x, group = bid & 7, split = bid >> 3;
  const int tid = threadIdx.x, lane = tid & 63, wave = __builtin_amdgcn_readfirstlane(tid >> 6), g = lane >> 4, c = lane & 15;
  float* stat = reinterpret_cast<float*>(smem + LDS_STAT);
  constexpr unsigned X = PSTR - 1024u;
  const unsigned v0 = g * 256 + ((c ^ (4 * g + 0)) << 4), v1 = X + g * 256 + ((c ^ (4 * g + 1)) << 4), v2 = 2 * X + g * 256 + ((c ^ (4 * g + 2)) << 4), v3 = 3 * X + g * 256 + ((c ^ (4 * g + 3)) << 4);
  const unsigned kvbase = (unsigned)(group * CTX + split * KEYS_WG) * (D * 2) + (unsigned)wave * WSTR;
  const unsigned lds0 = (unsigned)(uintptr_t)(LAS unsigned char*)smem + (unsigned)wave * WREG;
  asm volatile("" :: "v"(v0), "v"(v1), "v"(v2), "v"(v3), "s"(kvbase), "s"(lds0));
  asm volatile("" :: "s"(q), "s"(kc), "s"(vc), "s"(ws));
  if (__builtin_expect(wave < NWAVE, 1)) {
    const __amdgpu_buffer_rsrc_t rk = __builtin_amdgcn_make_buffer_rsrc((void*)kc, 0, (int)KV_BYTES, 0x00020000);
#pragma unroll
    for (int t = 0; t < 4; ++t)
      asm volatile("s_mov_b32 m0, %0\n\ts_nop 0\n\t"
                   "buffer_load_dwordx4 %1, %5, %6 offen nt lds\n\t"
                   "buffer_load_dwordx4 %2, %5, %6 offen offset:1024 nt lds\n\t"
                   "buffer_load_dwordx4 %3, %5, %6 offen offset:2048 nt lds\n\t"
                   "buffer_load_dwordx4 %4, %5, %6 offen offset:3072 nt lds"
                   :: "s"(lds0 + t * 4096), "v"(v0), "v"(v1), "v"(v2), "v"(v3), "s"(rk), "s"(kvbase + t * 4 * PSTR) : "m0", "memory");
    u32x4 Q[4];
    {
      const unsigned short* qp = q + (size_t)(group * REP + (c & 7)) * D + g * 8;
#pragma unroll
      for (int s = 0; s < 4; ++s) Q[s] = *reinterpret_cast<const u32x4*>(qp + s * 32);
    }
    __builtin_amdgcn_sched_barrier(0);
    const __amdgpu_buffer_rsrc_t rv = __builtin_amdgcn_make_buffer_rsrc((void*)vc, 0, (int)KV_BYTES, 0x00020000);
    u32x4 V[16];
#pragma unroll
    for (int n = 0; n < 16; ++n) V[n] = __builtin_amdgcn_raw_buffer_load_b128(rv, lane * 16, kvbase + n * PSTR, 2  );
    __builtin_amdgcn_sched_barrier(0);
    asm volatile("s_waitcnt vmcnt(16)" ::: "memory");
    __builtin_amdgcn_sched_barrier(0);
    f32x4 S[4];
    {
      const int row = 4 * (c & 3) + (c >> 2);
      const unsigned char* kb = smem + wave * WREG + row * 256;
#pragma unroll
      for (int t = 0; t < 4; ++t) {
        f32x4 a = {0.f, 0.f, 0.f, 0.f};
#pragma unroll
        for (int s = 0; s < 4; ++s) {
          const u32x4 kf = *reinterpret_cast<const u32x4*>(kb + t * 4096 + (((4 * s + g) ^ c) << 4));
          a = mfma16(kf, Q[s], a);
        }
        S[t] = a;
      }
    }
    float m = S[0][0];
#pragma unroll
    for (int t = 0; t < 4; ++t)
#pragma unroll
      for (int r = 0; r < 4; ++r) m = fmaxf(m, S[t][r]);
    m = xrow16_max(m);
    const float mc = m * C2;
    float l = 0.f;
    u32x4 P[2];
#pragma unroll
    for (int u = 0; u < 2; ++u) {
      float p[8];
#pragma unroll
      for (int j = 0; j < 8; ++j) { p[j] = __builtin_amdgcn_exp2f(fmaf(S[2 * u + (j >> 2)][j & 3], C2, -mc)); l += p[j]; }
#pragma unroll
      for (int i = 0; i < 4; ++i) P[u][i] = cvtpk(p[2 * i], p[2 * i + 1]);
    }
    l = xrow16_sum(l);
    if (lane < 8) { stat[lane * NWAVE + wave] = mc; stat[64 + lane * NWAVE + wave] = l; }
    __builtin_amdgcn_sched_barrier(0);
    const int h = wave;
    const __amdgpu_buffer_rsrc_t rw = __builtin_amdgcn_make_buffer_rsrc((void*)(ws + (size_t)group * WS_GROUP), 0, (int)WS_GROUP, 0x00020000);
    LAS unsigned char* lo_ = (LAS unsigned char*)smem;
    unsigned roff = WS_REC + ((4 * h + g) * NSPLIT + split) * 128 + c * 8;
    unsigned ao = (h * OSTR + 2 * lane) * 4, ao2 = ao + 4 * PREG, as = LDS_STAT + h * (NWAVE * 4), sh = 2 * c, aw = wave * PREG + (c * OSTR + 4 * g) * 4;
    asm volatile("" : "+v"(roff), "+v"(ao), "+v"(ao2), "+v"(as), "+v"(sh), "+v"(aw) : "s"(rw));
    __builtin_amdgcn_sched_barrier(0);
    f32x4 acc[8];
#pragma unroll
    for (int u = 0; u < 2; ++u) {
#pragma unroll
      for (int e = 0; e < 8; ++e) {
        u32x4 A;
#pragma unroll
        for (int i = 0; i < 4; ++i) A[i] = __builtin_amdgcn_perm(V[8 * u + 2 * i + 1][e >> 1], V[8 * u + 2 * i][e >> 1], (e & 1) ? 0x07060302u : 0x05040100u);
        acc[e] = mfma16(A, P[u], u == 0 ? f32x4{0.f, 0.f, 0.f, 0.f} : acc[e]);
      }
      __builtin_amdgcn_sched_group_barrier(0x2, 8, 0); __builtin_amdgcn_sched_group_barrier(0x8, 1, 0);
#pragma unroll
      for (int e = 1; e < 8; ++e) { __builtin_amdgcn_sched_group_barrier(0x2, 4, 0); __builtin_amdgcn_sched_group_barrier(0x8, 1, 0); }
      __builtin_amdgcn_sched_barrier(0);
    }
    if (c < 8) {
#pragma unroll
      for (int e = 0; e < 8; ++e) *(LAS f32x4*)(lo_ + aw + 64 * e) = acc[e];
    }
    asm volatile("s_waitcnt lgkmcnt(0)\n\ts_barrier" ::: "memory");
    {
      const f32x4 m0 = *(const LAS f32x4*)(lo_ + as), m1 = *(const LAS f32x4*)(lo_ + as + 16);
      const f32x4 l0 = *(const LAS f32x4*)(lo_ + as + 256), l1 = *(const LAS f32x4*)(lo_ + as + 272);
      const unsigned tag = *(const LAS unsigned*)(lo_ + LDS_STAT + 512);
      __builtin_amdgcn_sched_barrier(0);
      f32x2 ov[NWAVE];
#pragma unroll
      for (int w = 0; w < NWAVE; ++w) ov[w] = *(const LAS f32x2*)(lo_ + (w < 4 ? ao : ao2) + (w & 3) * PREG);
      __builtin_amdgcn_sched_barrier(0);
      const float Mh = max3f(max3f(m0[0], m0[1], m0[2]), max3f(m0[3], m1[0], m1[1]), max2f(m1[2], m1[3]));
      float f[NWAVE];
#pragma unroll
      for (int w = 0; w < 4; ++w) { f[w] = __builtin_amdgcn_exp2f(m0[w] - Mh); f[4 + w] = __builtin_amdgcn_exp2f(m1[w] - Mh); }
      const float L = (fmaf(f[1], l0[1], f[0] * l0[0]) + fmaf(f[3], l0[3], f[2] * l0[2])) + (fmaf(f[5], l1[1], f[4] * l1[0]) + fmaf(f[7], l1[3], f[6] * l1[2]));
      const float inv = __builtin_amdgcn_rcpf(L);
      const unsigned lse = __float_as_uint(Mh + __builtin_amdgcn_logf(L));
      const unsigned lt = (((lse >> sh) & 3u) << 18) | tag;
      const f32x2 ot = ((f[0] * ov[0] + f[1] * ov[1]) + (f[2] * ov[2] + f[3] * ov[3])) + ((f[4] * ov[4] + f[5] * ov[5]) + (f[6] * ov[6] + f[7] * ov[7]));
      const unsigned a = (__float_as_uint(ot[0] * inv) + 0x200u) & 0xfffffc00u, b = __float_as_uint(ot[1] * inv) + 0x200u;
      const unsigned hi = a | (b >> 22), lo = ((b << 10) & 0xfff00000u) | lt;
      __builtin_amdgcn_raw_buffer_store_b64(u32x2{lo, hi}, rw, roff, 0, 16  );
    }
    return;
  }

  {
    const __amdgpu_buffer_rsrc_t rw = __builtin_amdgcn_make_buffer_rsrc((void*)(ws + (size_t)group * WS_GROUP), 0, (int)WS_GROUP, 0x00020000);
    const int ch = split >> 2, cq = split & 3, p = lane & 7;
    const unsigned long long gen = __hip_atomic_load((GAS unsigned long long*)(ws + (size_t)group * WS_GROUP), __ATOMIC_RELAXED, __HIP_MEMORY_SCOPE_AGENT);
    const unsigned tq = *reinterpret_cast<const unsigned*>(q + (size_t)(group * REP + ch) * D + 2 * lane);
    const unsigned tk = *reinterpret_cast<const unsigned*>(kt + (size_t)group * D + 2 * lane);
    const int dout = 32 * (p & 3) + 8 * g + 2 * cq + (p >> 2);
    const unsigned tv = vt[(size_t)group * D + dout];
    const unsigned tmask = tm[group >> 2];
    GAS unsigned short* outp = (GAS unsigned short*)out + (size_t)(group * REP + ch) * D + dout;
    {
      u32x4 x[4];
#pragma unroll
      for (int k = 0; k < 4; ++k) x[k] = __builtin_amdgcn_raw_buffer_load_b128(rw, WS_REC + split * (NSPLIT * 128) + lane * 16 + k * 1024, 0, 16);
      const unsigned y = __hip_atomic_load(outp, __ATOMIC_RELAXED, __HIP_MEMORY_SCOPE_WORKGROUP);
      asm volatile("" :: "v"(x[0]), "v"(x[1]), "v"(x[2]), "v"(x[3]), "v"(y));
    }
    float st = bflo(tq) * bflo(tk) + bfhi(tq) * bfhi(tk);
    st = xrow16_sum(row16_sum(st));
    float mt = st * C2 + (1.0f - bflo(tmask)) * NEG2;
    float tvf = bflo(tv);
    const unsigned tag = __builtin_amdgcn_readfirstlane(((unsigned)gen + 1u) & TAGM);
    asm volatile("" : "+v"(tvf), "+v"(mt));
    if (lane == 0) reinterpret_cast<unsigned*>(stat)[128] = tag;
    asm volatile("s_waitcnt lgkmcnt(0)\n\ts_barrier" ::: "memory");
    __builtin_amdgcn_s_sleep(POLL_FIRST);
    u32x4 o[4];
    bool good = false;
    for (unsigned spin = 0; spin < SPIN_CAP; ++spin) {
      asm volatile("" ::: "memory");
#pragma unroll
      for (int k = 0; k < 4; ++k) o[k] = __builtin_amdgcn_raw_buffer_load_b128(rw, WS_REC + split * (NSPLIT * 128) + lane * 16 + k * 1024, 0, 16  );
      unsigned bad = 0;
#pragma unroll
      for (int k = 0; k < 4; ++k) bad |= (o[k][0] ^ tag) | (o[k][2] ^ tag);
      if (__all((bad & TAGM) == 0)) { good = true; break; }
      __builtin_amdgcn_s_sleep(POLL_SLEEP);
    }
    float lse[4], M = mt;
#pragma unroll
    for (int k = 0; k < 4; ++k) {
      unsigned x = (((o[k][0] >> 18) & 3u) | ((o[k][2] >> 16) & 12u)) << (4 * p);
      x |= dppu<XOR1>(x); x |= dppu<XOR2>(x); x |= dppu<HMIR>(x);
      lse[k] = __uint_as_float(x);
      M = max2f(M, lse[k]);
    }
    M = xrow16_max(max2f(M, dppf<ROR8>(M)));
    float L = 0.f, a0 = 0.f, a1 = 0.f, a2 = 0.f, a3 = 0.f;
#pragma unroll
    for (int k = 0; k < 4; ++k) {
      const float w = __builtin_amdgcn_exp2f(lse[k] - M);
      L += w;
      a0 = fmaf(w, __uint_as_float(o[k][1] & 0xfffffc00u), a0);
      a1 = fmaf(w, __uint_as_float(__builtin_amdgcn_alignbit(o[k][1], o[k][0], 10) & 0xfffffc00u), a1);
      a2 = fmaf(w, __uint_as_float(o[k][3] & 0xfffffc00u), a2);
      a3 = fmaf(w, __uint_as_float(__builtin_amdgcn_alignbit(o[k][3], o[k][2], 10) & 0xfffffc00u), a3);
    }
    const float wt = __builtin_amdgcn_exp2f(mt - M);
    auto t01 = __builtin_amdgcn_permlane16_swap(__float_as_uint(a0), __float_as_uint(a1), false, false);
    auto t23 = __builtin_amdgcn_permlane16_swap(__float_as_uint(a2), __float_as_uint(a3), false, false);
    const float b0 = __uint_as_float(t01[0]) + __uint_as_float(t01[1]), b1 = __uint_as_float(t23[0]) + __uint_as_float(t23[1]);
    auto tb = __builtin_amdgcn_permlane32_swap(__float_as_uint(b0), __float_as_uint(b1), false, false);
    float sel = __uint_as_float(tb[0]) + __uint_as_float(tb[1]);
    sel += dppf<ROR8>(sel);
    L += dppf<ROR8>(L);
    L = xrow16_sum(L);
    const float inv = good ? __builtin_amdgcn_rcpf(L + wt) : __builtin_nanf("");
    const float res = fmaf(wt, tvf, sel) * inv;
    if ((lane & 8) == 0) *outp = (unsigned short)cvtpk(res, res);
    if (split == 0 && lane == 0 && good)
      __hip_atomic_store((GAS unsigned long long*)(ws + (size_t)group * WS_GROUP), gen + 1ull, __ATOMIC_RELAXED, __HIP_MEMORY_SCOPE_AGENT);
  }
}

extern "C" void kernel_launch(void* const* d_in, const int* in_sizes, int n_in, void* d_out, int out_size, void* d_ws, size_t ws_size,
                              hipStream_t stream) {
  (void)in_sizes; (void)n_in; (void)out_size; (void)ws_size;
  tree_attn_decode<<<256, 576, 0, stream>>>((const unsigned short*)d_in[0], (const unsigned short*)d_in[1], (const unsigned short*)d_in[2],
                                            (const unsigned short*)d_in[3], (const unsigned short*)d_in[4], (const unsigned short*)d_in[5],
                                            (unsigned short*)d_out, (unsigned char*)d_ws);
}
```

```cpp
#include <hip/hip_runtime.h>
#include <stdint.h>

typedef __attribute__((ext_vector_type(4))) unsigned u32x4;
typedef __attribute__((ext_vector_type(2))) unsigned u32x2;
typedef __attribute__((ext_vector_type(8))) __bf16 bf16x8;
typedef __attribute__((ext_vector_type(4))) float f32x4;
typedef __attribute__((ext_vector_type(2))) float f32x2;
#define LAS __attribute__((address_space(3)))
#define GAS __attribute__((address_space(1)))

constexpr int D = 128, CTX = 16384, REP = 8;
constexpr int NSPLIT = 32, KEYS_WG = 512, NWAVE = 8;
constexpr float C2 = 0.088388347648318440f * 1.4426950408889634f;
constexpr float NEG2 = -1.0e9f * 1.4426950408889634f;
constexpr unsigned KV_BYTES = 2u * 4u * CTX * D * 2u;
constexpr int WREG = 16384;
constexpr int PREG = WREG + 32;
constexpr int OSTR = 132;
constexpr int LDS_STAT = NWAVE * WREG;
constexpr unsigned PSTR = 8192u, WSTR = 1024u;
constexpr unsigned WS_GROUP = 256u * 1024u, WS_REC = 4096u;
constexpr unsigned TAGM = 0x3ffffu;
constexpr unsigned SPIN_CAP = 1u << 18;
constexpr int POLL_FIRST = 24, POLL_SLEEP = 4;

__device__ __forceinline__ f32x4 mfma16(u32x4 a, u32x4 b, f32x4 c) {
  return __builtin_amdgcn_mfma_f32_16x16x32_bf16(__builtin_bit_cast(bf16x8, a), __builtin_bit_cast(bf16x8, b), c, 0, 0, 0);
}
__device__ __forceinline__ unsigned cvtpk(float lo, float hi) {
  typedef __attribute__((ext_vector_type(2))) __bf16 bf16x2;
  bf16x2 r = {(__bf16)lo, (__bf16)hi};
  return __builtin_bit_cast(unsigned, r);
}
__device__ __forceinline__ float max2f(float a, float b) { return __builtin_amdgcn_fmed3f(a, b, 3.4028234e38f); }
__device__ __forceinline__ float max3f(float a, float b, float c) { return fmaxf(fmaxf(a, b), c); }
__device__ __forceinline__ float xrow16_max(float x) {
  auto s = __builtin_amdgcn_permlane16_swap(__float_as_uint(x), __float_as_uint(x), false, false);
  x = max2f(__uint_as_float(s[0]), __uint_as_float(s[1]));
  auto t = __builtin_amdgcn_permlane32_swap(__float_as_uint(x), __float_as_uint(x), false, false);
  return max2f(__uint_as_float(t[0]), __uint_as_float(t[1]));
}
__device__ __forceinline__ float xrow16_sum(float x) {
  auto s = __builtin_amdgcn_permlane16_swap(__float_as_uint(x), __float_as_uint(x), false, false);
  x = __uint_as_float(s[0]) + __uint_as_float(s[1]);
  auto t = __builtin_amdgcn_permlane32_swap(__float_as_uint(x), __float_as_uint(x), false, false);
  return __uint_as_float(t[0]) + __uint_as_float(t[1]);
}
template <int CTRL> __device__ __forceinline__ float dppf(float x) {
  return __builtin_bit_cast(float, __builtin_amdgcn_mov_dpp(__builtin_bit_cast(int, x), CTRL, 0xf, 0xf, true));
}
template <int CTRL> __device__ __forceinline__ unsigned dppu(unsigned x) { return (unsigned)__builtin_amdgcn_mov_dpp((int)x, CTRL, 0xf, 0xf, true); }
constexpr int XOR1 = 0xB1, XOR2 = 0x4E, HMIR = 0x141, ROR8 = 0x128;
__device__ __forceinline__ float row16_sum(float x) { x += dppf<XOR1>(x); x += dppf<XOR2>(x); x += dppf<HMIR>(x); x += dppf<ROR8>(x); return x; }
__device__ __forceinline__ float bflo(unsigned u) { return __uint_as_float(u << 16); }
__device__ __forceinline__ float bfhi(unsigned u) { return __uint_as_float(u & 0xffff0000u); }

__global__ void __launch_bounds__(576, 3) tree_attn_decode(const unsigned short* __restrict__ q, const unsigned short* __restrict__ kc,
                                                           const unsigned short* __restrict__ vc, const unsigned short* __restrict__ kt,
                                                           const unsigned short* __restrict__ vt, const unsigned short* __restrict__ tm,
                                                           unsigned short* __restrict__ out, unsigned char* __restrict__ ws) {
  __shared__ __attribute__((aligned(16))) unsigned char smem[NWAVE * WREG + 640];
  const int bid = blockIdx.x, group = bid & 7, split = bid >> 3;
  const int tid = threadIdx.x, lane = tid & 63, wave = __builtin_amdgcn_readfirstlane(tid >> 6), g = lane >> 4, c = lane & 15;
  float* stat = reinterpret_cast<float*>(smem + LDS_STAT);
  constexpr unsigned X = PSTR - 1024u;
  const unsigned v0 = g * 256 + ((c ^ (4 * g + 0)) << 4), v1 = X + g * 256 + ((c ^ (4 * g + 1)) << 4), v2 = 2 * X + g * 256 + ((c ^ (4 * g + 2)) << 4), v3 = 3 * X + g * 256 + ((c ^ (4 * g + 3)) << 4);
  const unsigned kvbase = (unsigned)(group * CTX + split * KEYS_WG) * (D * 2) + (unsigned)wave * WSTR;
  const unsigned lds0 = (unsigned)(uintptr_t)(LAS unsigned char*)smem + (unsigned)wave * WREG;
  asm volatile("" :: "v"(v0), "v"(v1), "v"(v2), "v"(v3), "s"(kvbase), "s"(lds0));
  asm volatile("" :: "s"(q), "s"(kc), "s"(vc), "s"(ws));
  if (__builtin_expect(wave < NWAVE, 1)) {
    const __amdgpu_buffer_rsrc_t rk = __builtin_amdgcn_make_buffer_rsrc((void*)kc, 0, (int)KV_BYTES, 0x00020000);
#pragma unroll
    for (int t = 0; t < 4; ++t)
      asm volatile("s_mov_b32 m0, %0\n\ts_nop 0\n\t"
                   "buffer_load_dwordx4 %1, %5, %6 offen lds\n\t"
                   "buffer_load_dwordx4 %2, %5, %6 offen offset:1024 lds\n\t"
                   "buffer_load_dwordx4 %3, %5, %6 offen offset:2048 lds\n\t"
                   "buffer_load_dwordx4 %4, %5, %6 offen offset:3072 lds"
                   :: "s"(lds0 + t * 4096), "v"(v0), "v"(v1), "v"(v2), "v"(v3), "s"(rk), "s"(kvbase + t * 4 * PSTR) : "m0", "memory");
    u32x4 Q[4];
    {
      const unsigned short* qp = q + (size_t)(group * REP + (c & 7)) * D + g * 8;
#pragma unroll
      for (int s = 0; s < 4; ++s) Q[s] = *reinterpret_cast<const u32x4*>(qp + s * 32);
    }
    __builtin_amdgcn_sched_barrier(0);
    const __amdgpu_buffer_rsrc_t rv = __builtin_amdgcn_make_buffer_rsrc((void*)vc, 0, (int)KV_BYTES, 0x00020000);
    u32x4 V[16];
#pragma unroll
    for (int n = 0; n < 16; ++n) V[n] = __builtin_amdgcn_raw_buffer_load_b128(rv, lane * 16, kvbase + n * PSTR, 0);
    __builtin_amdgcn_sched_barrier(0);
    asm volatile("s_waitcnt vmcnt(16)" ::: "memory");
    __builtin_amdgcn_sched_barrier(0);
    f32x4 S[4];
    {
      const int row = 4 * (c & 3) + (c >> 2);
      const unsigned char* kb = smem + wave * WREG + row * 256;
#pragma unroll
      for (int t = 0; t < 4; ++t) {
        f32x4 a = {0.f, 0.f, 0.f, 0.f};
#pragma unroll
        for (int s = 0; s < 4; ++s) {
          const u32x4 kf = *reinterpret_cast<const u32x4*>(kb + t * 4096 + (((4 * s + g) ^ c) << 4));
          a = mfma16(kf, Q[s], a);
        }
        S[t] = a;
      }
    }
    float m = S[0][0];
#pragma unroll
    for (int t = 0; t < 4; ++t)
#pragma unroll
      for (int r = 0; r < 4; ++r) m = fmaxf(m, S[t][r]);
    m = xrow16_max(m);
    const float mc = m * C2;
    float l = 0.f;
    u32x4 P[2];
#pragma unroll
    for (int u = 0; u < 2; ++u) {
      float p[8];
#pragma unroll
      for (int j = 0; j < 8; ++j) { p[j] = __builtin_amdgcn_exp2f(fmaf(S[2 * u + (j >> 2)][j & 3], C2, -mc)); l += p[j]; }
#pragma unroll
      for (int i = 0; i < 4; ++i) P[u][i] = cvtpk(p[2 * i], p[2 * i + 1]);
    }
    l = xrow16_sum(l);
    if (lane < 8) { stat[lane * NWAVE + wave] = mc; stat[64 + lane * NWAVE + wave] = l; }
    __builtin_amdgcn_sched_barrier(0);
    const int h = wave;
    const __amdgpu_buffer_rsrc_t rw = __builtin_amdgcn_make_buffer_rsrc((void*)(ws + (size_t)group * WS_GROUP), 0, (int)WS_GROUP, 0x00020000);
    LAS unsigned char* lo_ = (LAS unsigned char*)smem;
    unsigned roff = WS_REC + ((4 * h + g) * NSPLIT + split) * 128 + c * 8;
    unsigned ao = (h * OSTR + 2 * lane) * 4, ao2 = ao + 4 * PREG, as = LDS_STAT + h * (NWAVE * 4), sh = 2 * c, aw = wave * PREG + (c * OSTR + 4 * g) * 4;
    asm volatile("" : "+v"(roff), "+v"(ao), "+v"(ao2), "+v"(as), "+v"(sh), "+v"(aw) : "s"(rw));
    __builtin_amdgcn_sched_barrier(0);
    f32x4 acc[8];
#pragma unroll
    for (int u = 0; u < 2; ++u) {
#pragma unroll
      for (int e = 0; e < 8; ++e) {
        u32x4 A;
#pragma unroll
        for (int i = 0; i < 4; ++i) A[i] = __builtin_amdgcn_perm(V[8 * u + 2 * i + 1][e >> 1], V[8 * u + 2 * i][e >> 1], (e & 1) ? 0x07060302u : 0x05040100u);
        acc[e] = mfma16(A, P[u], u == 0 ? f32x4{0.f, 0.f, 0.f, 0.f} : acc[e]);
      }
      __builtin_amdgcn_sched_group_barrier(0x2, 8, 0); __builtin_amdgcn_sched_group_barrier(0x8, 1, 0);
#pragma unroll
      for (int e = 1; e < 8; ++e) { __builtin_amdgcn_sched_group_barrier(0x2, 4, 0); __builtin_amdgcn_sched_group_barrier(0x8, 1, 0); }
      __builtin_amdgcn_sched_barrier(0);
    }
    if (c < 8) {
#pragma unroll
      for (int e = 0; e < 8; ++e) *(LAS f32x4*)(lo_ + aw + 64 * e) = acc[e];
    }
    asm volatile("s_waitcnt lgkmcnt(0)\n\ts_barrier" ::: "memory");
    {
      const f32x4 m0 = *(const LAS f32x4*)(lo_ + as), m1 = *(const LAS f32x4*)(lo_ + as + 16);
      const f32x4 l0 = *(const LAS f32x4*)(lo_ + as + 256), l1 = *(const LAS f32x4*)(lo_ + as + 272);
      const unsigned tag = *(const LAS unsigned*)(lo_ + LDS_STAT + 512);
      __builtin_amdgcn_sched_barrier(0);
      f32x2 ov[NWAVE];
#pragma unroll
      for (int w = 0; w < NWAVE; ++w) ov[w] = *(const LAS f32x2*)(lo_ + (w < 4 ? ao : ao2) + (w & 3) * PREG);
      __builtin_amdgcn_sched_barrier(0);
      const float Mh = max3f(max3f(m0[0], m0[1], m0[2]), max3f(m0[3], m1[0], m1[1]), max2f(m1[2], m1[3]));
      float f[NWAVE];
#pragma unroll
      for (int w = 0; w < 4; ++w) { f[w] = __builtin_amdgcn_exp2f(m0[w] - Mh); f[4 + w] = __builtin_amdgcn_exp2f(m1[w] - Mh); }
      const float L = (fmaf(f[1], l0[1], f[0] * l0[0]) + fmaf(f[3], l0[3], f[2] * l0[2])) + (fmaf(f[5], l1[1], f[4] * l1[0]) + fmaf(f[7], l1[3], f[6] * l1[2]));
      const float inv = __builtin_amdgcn_rcpf(L);
      const unsigned lse = __float_as_uint(Mh + __builtin_amdgcn_logf(L));
      const unsigned lt = (((lse >> sh) & 3u) << 18) | tag;
      const f32x2 ot = ((f[0] * ov[0] + f[1] * ov[1]) + (f[2] * ov[2] + f[3] * ov[3])) + ((f[4] * ov[4] + f[5] * ov[5]) + (f[6] * ov[6] + f[7] * ov[7]));
      const unsigned a = (__float_as_uint(ot[0] * inv) + 0x200u) & 0xfffffc00u, b = __float_as_uint(ot[1] * inv) + 0x200u;
      const unsigned hi = a | (b >> 22), lo = ((b << 10) & 0xfff00000u) | lt;
      const unsigned lo2 = dppu<0xF5>(lo), hi2 = dppu<0xF5>(hi);
      if ((c & 1) == 0) __builtin_amdgcn_raw_buffer_store_b128(u32x4{lo, hi, lo2, hi2}, rw, roff, 0, 16  );
    }
    return;
  }

  {
    const __amdgpu_buffer_rsrc_t rw = __builtin_amdgcn_make_buffer_rsrc((void*)(ws + (size_t)group * WS_GROUP), 0, (int)WS_GROUP, 0x00020000);
    const int ch = split >> 2, cq = split & 3, p = lane & 7;
    const unsigned long long gen = __hip_atomic_load((GAS unsigned long long*)(ws + (size_t)group * WS_GROUP), __ATOMIC_RELAXED, __HIP_MEMORY_SCOPE_AGENT);
    const unsigned tq = *reinterpret_cast<const unsigned*>(q + (size_t)(group * REP + ch) * D + 2 * lane);
    const unsigned tk = *reinterpret_cast<const unsigned*>(kt + (size_t)group * D + 2 * lane);
    const int dout = 32 * (p & 3) + 8 * g + 2 * cq + (p >> 2);
    const unsigned tv = vt[(size_t)group * D + dout];
    const unsigned tmask = tm[group >> 2];
    GAS unsigned short* outp = (GAS unsigned short*)out + (size_t)(group * REP + ch) * D + dout;
    {
      u32x4 x[4];
#pragma unroll
      for (int k = 0; k < 4; ++k) x[k] = __builtin_amdgcn_raw_buffer_load_b128(rw, WS_REC + split * (NSPLIT * 128) + lane * 16 + k * 1024, 0, 16);
      const unsigned y = __hip_atomic_load(outp, __ATOMIC_RELAXED, __HIP_MEMORY_SCOPE_WORKGROUP);
      asm volatile("" :: "v"(x[0]), "v"(x[1]), "v"(x[2]), "v"(x[3]), "v"(y));
    }
    float st = bflo(tq) * bflo(tk) + bfhi(tq) * bfhi(tk);
    st = xrow16_sum(row16_sum(st));
    float mt = st * C2 + (1.0f - bflo(tmask)) * NEG2;
    float tvf = bflo(tv);
    const unsigned tag = __builtin_amdgcn_readfirstlane(((unsigned)gen + 1u) & TAGM);
    asm volatile("" : "+v"(tvf), "+v"(mt));
    if (lane == 0) reinterpret_cast<unsigned*>(stat)[128] = tag;
    asm volatile("s_waitcnt lgkmcnt(0)\n\ts_barrier" ::: "memory");
    __builtin_amdgcn_s_sleep(POLL_FIRST);
    u32x4 o[4];
    bool good = false;
    for (unsigned spin = 0; spin < SPIN_CAP; ++spin) {
      asm volatile("" ::: "memory");
#pragma unroll
      for (int k = 0; k < 4; ++k) o[k] = __builtin_amdgcn_raw_buffer_load_b128(rw, WS_REC + split * (NSPLIT * 128) + lane * 16 + k * 1024, 0, 16  );
      unsigned bad = 0;
#pragma unroll
      for (int k = 0; k < 4; ++k) bad |= (o[k][0] ^ tag) | (o[k][2] ^ tag);
      if (__all((bad & TAGM) == 0)) { good = true; break; }
      __builtin_amdgcn_s_sleep(POLL_SLEEP);
    }
    float lse[4], M = mt;
#pragma unroll
    for (int k = 0; k < 4; ++k) {
      unsigned x = (((o[k][0] >> 18) & 3u) | ((o[k][2] >> 16) & 12u)) << (4 * p);
      x |= dppu<XOR1>(x); x |= dppu<XOR2>(x); x |= dppu<HMIR>(x);
      lse[k] = __uint_as_float(x);
      M = max2f(M, lse[k]);
    }
    M = xrow16_max(max2f(M, dppf<ROR8>(M)));
    float L = 0.f, a0 = 0.f, a1 = 0.f, a2 = 0.f, a3 = 0.f;
#pragma unroll
    for (int k = 0; k < 4; ++k) {
      const float w = __builtin_amdgcn_exp2f(lse[k] - M);
      L += w;
      a0 = fmaf(w, __uint_as_float(o[k][1] & 0xfffffc00u), a0);
      a1 = fmaf(w, __uint_as_float(__builtin_amdgcn_alignbit(o[k][1], o[k][0], 10) & 0xfffffc00u), a1);
      a2 = fmaf(w, __uint_as_float(o[k][3] & 0xfffffc00u), a2);
      a3 = fmaf(w, __uint_as_float(__builtin_amdgcn_alignbit(o[k][3], o[k][2], 10) & 0xfffffc00u), a3);
    }
    const float wt = __builtin_amdgcn_exp2f(mt - M);
    auto t01 = __builtin_amdgcn_permlane16_swap(__float_as_uint(a0), __float_as_uint(a1), false, false);
    auto t23 = __builtin_amdgcn_permlane16_swap(__float_as_uint(a2), __float_as_uint(a3), false, false);
    const float b0 = __uint_as_float(t01[0]) + __uint_as_float(t01[1]), b1 = __uint_as_float(t23[0]) + __uint_as_float(t23[1]);
    auto tb = __builtin_amdgcn_permlane32_swap(__float_as_uint(b0), __float_as_uint(b1), false, false);
    float sel = __uint_as_float(tb[0]) + __uint_as_float(tb[1]);
    sel += dppf<ROR8>(sel);
    L += dppf<ROR8>(L);
    L = xrow16_sum(L);
    const float inv = good ? __builtin_amdgcn_rcpf(L + wt) : __builtin_nanf("");
    const float res = fmaf(wt, tvf, sel) * inv;
    if ((lane & 8) == 0) *outp = (unsigned short)cvtpk(res, res);
    if (split == 0 && lane == 0 && good)
      __hip_atomic_store((GAS unsigned long long*)(ws + (size_t)group * WS_GROUP), gen + 1ull, __ATOMIC_RELAXED, __HIP_MEMORY_SCOPE_AGENT);
  }
}

extern "C" void kernel_launch(void* const* d_in, const int* in_sizes, int n_in, void* d_out, int out_size, void* d_ws, size_t ws_size,
                              hipStream_t stream) {
  (void)in_sizes; (void)n_in; (void)out_size; (void)ws_size;
  tree_attn_decode<<<256, 576, 0, stream>>>((const unsigned short*)d_in[0], (const unsigned short*)d_in[1], (const unsigned short*)d_in[2],
                                            (const unsigned short*)d_in[3], (const unsigned short*)d_in[4], (const unsigned short*)d_in[5],
                                            (unsigned short*)d_out, (unsigned char*)d_ws);
}
```

```cpp
#include <hip/hip_runtime.h>
#include <stdint.h>

typedef __attribute__((ext_vector_type(4))) unsigned u32x4;
typedef __attribute__((ext_vector_type(2))) unsigned u32x2;
typedef __attribute__((ext_vector_type(8))) __bf16 bf16x8;
typedef __attribute__((ext_vector_type(4))) float f32x4;
typedef __attribute__((ext_vector_type(2))) float f32x2;
#define LAS __attribute__((address_space(3)))
#define GAS __attribute__((address_space(1)))

constexpr int D = 128, CTX = 16384, REP = 8;
constexpr int NSPLIT = 32, KEYS_WG = 512, NWAVE = 8;
constexpr float C2 = 0.088388347648318440f * 1.4426950408889634f;
constexpr float NEG2 = -1.0e9f * 1.4426950408889634f;
constexpr unsigned KV_BYTES = 2u * 4u * CTX * D * 2u;
constexpr int WREG = 16384;
constexpr int PREG = WREG + 32;
constexpr int OSTR = 132;
constexpr int LDS_STAT = NWAVE * WREG;
constexpr unsigned PSTR = 8192u, WSTR = 1024u;
constexpr unsigned WS_GROUP = 256u * 1024u, WS_REC = 4096u;
constexpr unsigned TAGM = 0x3ffffu;
constexpr unsigned SPIN_CAP = 1u << 18;
constexpr int POLL_FIRST = 24, POLL_SLEEP = 4;

__device__ __forceinline__ f32x4 mfma16(u32x4 a, u32x4 b, f32x4 c) {
  return __builtin_amdgcn_mfma_f32_16x16x32_bf16(__builtin_bit_cast(bf16x8, a), __builtin_bit_cast(bf16x8, b), c, 0, 0, 0);
}
__device__ __forceinline__ unsigned cvtpk(float lo, float hi) {
  typedef __attribute__((ext_vector_type(2))) __bf16 bf16x2;
  bf16x2 r = {(__bf16)lo, (__bf16)hi};
  return __builtin_bit_cast(unsigned, r);
}
__device__ __forceinline__ float max2f(float a, float b) { return __builtin_amdgcn_fmed3f(a, b, 3.4028234e38f); }
__device__ __forceinline__ float max3f(float a, float b, float c) { return fmaxf(fmaxf(a, b), c); }
__device__ __forceinline__ float xrow16_max(float x) {
  auto s = __builtin_amdgcn_permlane16_swap(__float_as_uint(x), __float_as_uint(x), false, false);
  x = max2f(__uint_as_float(s[0]), __uint_as_float(s[1]));
  auto t = __builtin_amdgcn_permlane32_swap(__float_as_uint(x), __float_as_uint(x), false, false);
  return max2f(__uint_as_float(t[0]), __uint_as_float(t[1]));
}
__device__ __forceinline__ float xrow16_sum(float x) {
  auto s = __builtin_amdgcn_permlane16_swap(__float_as_uint(x), __float_as_uint(x), false, false);
  x = __uint_as_float(s[0]) + __uint_as_float(s[1]);
  auto t = __builtin_amdgcn_permlane32_swap(__float_as_uint(x), __float_as_uint(x), false, false);
  return __uint_as_float(t[0]) + __uint_as_float(t[1]);
}
template <int CTRL> __device__ __forceinline__ float dppf(float x) {
  return __builtin_bit_cast(float, __builtin_amdgcn_mov_dpp(__builtin_bit_cast(int, x), CTRL, 0xf, 0xf, true));
}
template <int CTRL> __device__ __forceinline__ unsigned dppu(unsigned x) { return (unsigned)__builtin_amdgcn_mov_dpp((int)x, CTRL, 0xf, 0xf, true); }
constexpr int XOR1 = 0xB1, XOR2 = 0x4E, HMIR = 0x141, ROR8 = 0x128;
__device__ __forceinline__ float row16_sum(float x) { x += dppf<XOR1>(x); x += dppf<XOR2>(x); x += dppf<HMIR>(x); x += dppf<ROR8>(x); return x; }
__device__ __forceinline__ float bflo(unsigned u) { return __uint_as_float(u << 16); }
__device__ __forceinline__ float bfhi(unsigned u) { return __uint_as_float(u & 0xffff0000u); }

__global__ void __launch_bounds__(576, 3) tree_attn_decode(const unsigned short* __restrict__ q, const unsigned short* __restrict__ kc,
                                                           const unsigned short* __restrict__ vc, const unsigned short* __restrict__ kt,
                                                           const unsigned short* __restrict__ vt, const unsigned short* __restrict__ tm,
                                                           unsigned short* __restrict__ out, unsigned char* __restrict__ ws) {
  __shared__ __attribute__((aligned(16))) unsigned char smem[NWAVE * WREG + 640];
  const int bid = blockIdx.x, group = bid & 7, split = bid >> 3;
  const int tid = threadIdx.x, lane = tid & 63, wave = __builtin_amdgcn_readfirstlane(tid >> 6), g = lane >> 4, c = lane & 15;
  float* stat = reinterpret_cast<float*>(smem + LDS_STAT);
  constexpr unsigned X = PSTR - 1024u;
  const unsigned v0 = g * 256 + ((c ^ (4 * g + 0)) << 4), v1 = X + g * 256 + ((c ^ (4 * g + 1)) << 4), v2 = 2 * X + g * 256 + ((c ^ (4 * g + 2)) << 4), v3 = 3 * X + g * 256 + ((c ^ (4 * g + 3)) << 4);
  const unsigned kvbase = (unsigned)(group * CTX + split * KEYS_WG) * (D * 2) + (unsigned)wave * WSTR;
  const unsigned lds0 = (unsigned)(uintptr_t)(LAS unsigned char*)smem + (unsigned)wave * WREG;
  asm volatile("" :: "v"(v0), "v"(v1), "v"(v2), "v"(v3), "s"(kvbase), "s"(lds0));
  asm volatile("" :: "s"(q), "s"(kc), "s"(vc), "s"(ws));
  if (__builtin_expect(wave < NWAVE, 1)) {
    const __amdgpu_buffer_rsrc_t rk = __builtin_amdgcn_make_buffer_rsrc((void*)kc, 0, (int)KV_BYTES, 0x00020000);
#pragma unroll
    for (int t = 0; t < 4; ++t)
      asm volatile("s_mov_b32 m0, %0\n\ts_nop 0\n\t"
                   "buffer_load_dwordx4 %1, %5, %6 offen nt lds\n\t"
                   "buffer_load_dwordx4 %2, %5, %6 offen offset:1024 nt lds\n\t"
                   "buffer_load_dwordx4 %3, %5, %6 offen offset:2048 nt lds\n\t"
                   "buffer_load_dwordx4 %4, %5, %6 offen offset:3072 nt lds"
                   :: "s"(lds0 + t * 4096), "v"(v0), "v"(v1), "v"(v2), "v"(v3), "s"(rk), "s"(kvbase + t * 4 * PSTR) : "m0", "memory");
    u32x4 Q[4];
    {
      const unsigned short* qp = q + (size_t)(group * REP + (c & 7)) * D + g * 8;
#pragma unroll
      for (int s = 0; s < 4; ++s) Q[s] = *reinterpret_cast<const u32x4*>(qp + s * 32);
    }
    __builtin_amdgcn_sched_barrier(0);
    const __amdgpu_buffer_rsrc_t rv = __builtin_amdgcn_make_buffer_rsrc((void*)vc, 0, (int)KV_BYTES, 0x00020000);
    u32x4 V[16];
#pragma unroll
    for (int n = 0; n < 16; ++n) V[n] = __builtin_amdgcn_raw_buffer_load_b128(rv, lane * 16, kvbase + n * PSTR, 2  );
    __builtin_amdgcn_sched_barrier(0);
    asm volatile("s_waitcnt vmcnt(16)" ::: "memory");
    __builtin_amdgcn_sched_barrier(0);
    f32x4 S[4];
    {
      const int row = 4 * (c & 3) + (c >> 2);
      const unsigned char* kb = smem + wave * WREG + row * 256;
#pragma unroll
      for (int t = 0; t < 4; ++t) {
        f32x4 a = {0.f, 0.f, 0.f, 0.f};
#pragma unroll
        for (int s = 0; s < 4; ++s) {
          const u32x4 kf = *reinterpret_cast<const u32x4*>(kb + t * 4096 + (((4 * s + g) ^ c) << 4));
          a = mfma16(kf, Q[s], a);
        }
        S[t] = a;
      }
    }
    float m = S[0][0];
#pragma unroll
    for (int t = 0; t < 4; ++t)
#pragma unroll
      for (int r = 0; r < 4; ++r) m = fmaxf(m, S[t][r]);
    m = xrow16_max(m);
    const float mc = m * C2;
    float l = 0.f;
    u32x4 P[2];
#pragma unroll
    for (int u = 0; u < 2; ++u) {
      float p[8];
#pragma unroll
      for (int j = 0; j < 8; ++j) { p[j] = __builtin_amdgcn_exp2f(fmaf(S[2 * u + (j >> 2)][j & 3], C2, -mc)); l += p[j]; }
#pragma unroll
      for (int i = 0; i < 4; ++i) P[u][i] = cvtpk(p[2 * i], p[2 * i + 1]);
    }
    l = xrow16_sum(l);
    if (lane < 8) { stat[lane * NWAVE + wave] = mc; stat[64 + lane * NWAVE + wave] = l; }
    __builtin_amdgcn_sched_barrier(0);
    const int h = wave;
    const __amdgpu_buffer_rsrc_t rw = __builtin_amdgcn_make_buffer_rsrc((void*)(ws + (size_t)group * WS_GROUP), 0, (int)WS_GROUP, 0x00020000);
    LAS unsigned char* lo_ = (LAS unsigned char*)smem;
    unsigned roff = WS_REC + ((4 * h + g) * NSPLIT + split) * 128 + c * 8;
    unsigned ao = (h * OSTR + 2 * lane) * 4, ao2 = ao + 4 * PREG, as = LDS_STAT + h * (NWAVE * 4), sh = 2 * c, aw = wave * PREG + (c * OSTR + 4 * g) * 4;
    asm volatile("" : "+v"(roff), "+v"(ao), "+v"(ao2), "+v"(as), "+v"(sh), "+v"(aw) : "s"(rw));
    __builtin_amdgcn_sched_barrier(0);
    f32x4 acc[8];
#pragma unroll
    for (int u = 0; u < 2; ++u) {
#pragma unroll
      for (int e = 0; e < 8; ++e) {
        u32x4 A;
#pragma unroll
        for (int i = 0; i < 4; ++i) A[i] = __builtin_amdgcn_perm(V[8 * u + 2 * i + 1][e >> 1], V[8 * u + 2 * i][e >> 1], (e & 1) ? 0x07060302u : 0x05040100u);
        acc[e] = mfma16(A, P[u], u == 0 ? f32x4{0.f, 0.f, 0.f, 0.f} : acc[e]);
      }
      __builtin_amdgcn_sched_group_barrier(0x2, 8, 0); __builtin_amdgcn_sched_group_barrier(0x8, 1, 0);
#pragma unroll
      for (int e = 1; e < 8; ++e) { __builtin_amdgcn_sched_group_barrier(0x2, 4, 0); __builtin_amdgcn_sched_group_barrier(0x8, 1, 0); }
      __builtin_amdgcn_sched_barrier(0);
    }
    if (c < 8) {
#pragma unroll
      for (int e = 0; e < 8; ++e) *(LAS f32x4*)(lo_ + aw + 64 * e) = acc[e];
    }
    asm volatile("s_waitcnt lgkmcnt(0)\n\ts_barrier" ::: "memory");
    {
      const f32x4 m0 = *(const LAS f32x4*)(lo_ + as), m1 = *(const LAS f32x4*)(lo_ + as + 16);
      const f32x4 l0 = *(const LAS f32x4*)(lo_ + as + 256), l1 = *(const LAS f32x4*)(lo_ + as + 272);
      const unsigned tag = *(const LAS unsigned*)(lo_ + LDS_STAT + 512);
      __builtin_amdgcn_sched_barrier(0);
      f32x2 ov[NWAVE];
#pragma unroll
      for (int w = 0; w < NWAVE; ++w) ov[w] = *(const LAS f32x2*)(lo_ + (w < 4 ? ao : ao2) + (w & 3) * PREG);
      __builtin_amdgcn_sched_barrier(0);
      const float Mh = max3f(max3f(m0[0], m0[1], m0[2]), max3f(m0[3], m1[0], m1[1]), max2f(m1[2], m1[3]));
      float f[NWAVE];
#pragma unroll
      for (int w = 0; w < 4; ++w) { f[w] = __builtin_amdgcn_exp2f(m0[w] - Mh); f[4 + w] = __builtin_amdgcn_exp2f(m1[w] - Mh); }
      const float L = (fmaf(f[1], l0[1], f[0] * l0[0]) + fmaf(f[3], l0[3], f[2] * l0[2])) + (fmaf(f[5], l1[1], f[4] * l1[0]) + fmaf(f[7], l1[3], f[6] * l1[2]));
      const float inv = __builtin_amdgcn_rcpf(L);
      const unsigned lse = __float_as_uint(Mh + __builtin_amdgcn_logf(L));
      const unsigned lt = (((lse >> sh) & 3u) << 18) | tag;
      const f32x2 ot = ((f[0] * ov[0] + f[1] * ov[1]) + (f[2] * ov[2] + f[3] * ov[3])) + ((f[4] * ov[4] + f[5] * ov[5]) + (f[6] * ov[6] + f[7] * ov[7]));
      const unsigned a = (__float_as_uint(ot[0] * inv) + 0x200u) & 0xfffffc00u, b = __float_as_uint(ot[1] * inv) + 0x200u;
      const unsigned hi = a | (b >> 22), lo = ((b << 10) & 0xfff00000u) | lt;
      const unsigned lo2 = dppu<0xF5>(lo), hi2 = dppu<0xF5>(hi);
      if ((c & 1) == 0) __builtin_amdgcn_raw_buffer_store_b128(u32x4{lo, hi, lo2, hi2}, rw, roff, 0, 16  );
    }
    return;
  }

  {
    const __amdgpu_buffer_rsrc_t rw = __builtin_amdgcn_make_buffer_rsrc((void*)(ws + (size_t)group * WS_GROUP), 0, (int)WS_GROUP, 0x00020000);
    const int ch = split >> 2, cq = split & 3, p = lane & 7;
    const unsigned long long gen = __hip_atomic_load((GAS unsigned long long*)(ws + (size_t)group * WS_GROUP), __ATOMIC_RELAXED, __HIP_MEMORY_SCOPE_AGENT);
    const unsigned tq = *reinterpret_cast<const unsigned*>(q + (size_t)(group * REP + ch) * D + 2 * lane);
    const unsigned tk = *reinterpret_cast<const unsigned*>(kt + (size_t)group * D + 2 * lane);
    const int dout = 32 * (p & 3) + 8 * g + 2 * cq + (p >> 2);
    const unsigned tv = vt[(size_t)group * D + dout];
    const unsigned tmask = tm[group >> 2];
    GAS unsigned short* outp = (GAS unsigned short*)out + (size_t)(group * REP + ch) * D + dout;
    {
      u32x4 x[4];
#pragma unroll
      for (int k = 0; k < 4; ++k) x[k] = __builtin_amdgcn_raw_buffer_load_b128(rw, WS_REC + split * (NSPLIT * 128) + lane * 16 + k * 1024, 0, 16);
      const unsigned y = __hip_atomic_load(outp, __ATOMIC_RELAXED, __HIP_MEMORY_SCOPE_WORKGROUP);
      asm volatile("" :: "v"(x[0]), "v"(x[1]), "v"(x[2]), "v"(x[3]), "v"(y));
    }
    float st = bflo(tq) * bflo(tk) + bfhi(tq) * bfhi(tk);
    st = xrow16_sum(row16_sum(st));
    float mt = st * C2 + (1.0f - bflo(tmask)) * NEG2;
    float tvf = bflo(tv);
    const unsigned tag = __builtin_amdgcn_readfirstlane(((unsigned)gen + 1u) & TAGM);
    asm volatile("" : "+v"(tvf), "+v"(mt));
    if (lane == 0) reinterpret_cast<unsigned*>(stat)[128] = tag;
    asm volatile("s_waitcnt lgkmcnt(0)\n\ts_barrier" ::: "memory");
    __builtin_amdgcn_s_sleep(POLL_FIRST);
    u32x4 o[4];
    bool good = false;
    for (unsigned spin = 0; spin < SPIN_CAP; ++spin) {
      asm volatile("" ::: "memory");
#pragma unroll
      for (int k = 0; k < 4; ++k) o[k] = __builtin_amdgcn_raw_buffer_load_b128(rw, WS_REC + split * (NSPLIT * 128) + lane * 16 + k * 1024, 0, 16  );
      unsigned bad = 0;
#pragma unroll
      for (int k = 0; k < 4; ++k) bad |= (o[k][0] ^ tag) | (o[k][2] ^ tag);
      if (__all((bad & TAGM) == 0)) { good = true; break; }
      __builtin_amdgcn_s_sleep(POLL_SLEEP);
    }
    float lse[4], M = mt;
#pragma unroll
    for (int k = 0; k < 4; ++k) {
      unsigned x = (((o[k][0] >> 18) & 3u) | ((o[k][2] >> 16) & 12u)) << (4 * p);
      x |= dppu<XOR1>(x); x |= dppu<XOR2>(x); x |= dppu<HMIR>(x);
      lse[k] = __uint_as_float(x);
      M = max2f(M, lse[k]);
    }
    M = xrow16_max(max2f(M, dppf<ROR8>(M)));
    float L = 0.f, a0 = 0.f, a1 = 0.f, a2 = 0.f, a3 = 0.f;
#pragma unroll
    for (int k = 0; k < 4; ++k) {
      const float w = __builtin_amdgcn_exp2f(lse[k] - M);
      L += w;
      a0 = fmaf(w, __uint_as_float(o[k][1] & 0xfffffc00u), a0);
      a1 = fmaf(w, __uint_as_float(__builtin_amdgcn_alignbit(o[k][1], o[k][0], 10) & 0xfffffc00u), a1);
      a2 = fmaf(w, __uint_as_float(o[k][3] & 0xfffffc00u), a2);
      a3 = fmaf(w, __uint_as_float(__builtin_amdgcn_alignbit(o[k][3], o[k][2], 10) & 0xfffffc00u), a3);
    }
    const float wt = __builtin_amdgcn_exp2f(mt - M);
    auto t01 = __builtin_amdgcn_permlane16_swap(__float_as_uint(a0), __float_as_uint(a1), false, false);
    auto t23 = __builtin_amdgcn_permlane16_swap(__float_as_uint(a2), __float_as_uint(a3), false, false);
    const float b0 = __uint_as_float(t01[0]) + __uint_as_float(t01[1]), b1 = __uint_as_float(t23[0]) + __uint_as_float(t23[1]);
    auto tb = __builtin_amdgcn_permlane32_swap(__float_as_uint(b0), __float_as_uint(b1), false, false);
    float sel = __uint_as_float(tb[0]) + __uint_as_float(tb[1]);
    sel += dppf<ROR8>(sel);
    L += dppf<ROR8>(L);
    L = xrow16_sum(L);
    const float inv = good ? __builtin_amdgcn_rcpf(L + wt) : __builtin_nanf("");
    const float res = fmaf(wt, tvf, sel) * inv;
    if ((lane & 8) == 0) *outp = (unsigned short)cvtpk(res, res);
    if (split == 0 && lane == 0 && good)
      __hip_atomic_store((GAS unsigned long long*)(ws + (size_t)group * WS_GROUP), gen + 1ull, __ATOMIC_RELAXED, __HIP_MEMORY_SCOPE_AGENT);
  }
}

extern "C" void kernel_launch(void* const* d_in, const int* in_sizes, int n_in, void* d_out, int out_size, void* d_ws, size_t ws_size,
                              hipStream_t stream) {
  (void)in_sizes; (void)n_in; (void)out_size; (void)ws_size;
  tree_attn_decode<<<256, 576, 0, stream>>>((const unsigned short*)d_in[0], (const unsigned short*)d_in[1], (const unsigned short*)d_in[2],
                                            (const unsigned short*)d_in[3], (const unsigned short*)d_in[4], (const unsigned short*)d_in[5],
                                            (unsigned short*)d_out, (unsigned char*)d_ws);
}
```
